```python
import torch

def _softmax(x, axis=-1):
    return torch.softmax(x, dim=axis)
def _gelu(x):
    return torch.nn.functional.gelu(x, approximate="tanh")
def _silu(x):
    return torch.nn.functional.silu(x)
def _sigmoid(x):
    return torch.sigmoid(x)
def _layernorm(x, axis=-1, eps=1e-5):
    m = x.mean(dim=axis, keepdim=True)
    v = x.var(dim=axis, keepdim=True, unbiased=False)
    return (x - m) / torch.sqrt(v + eps)
def _scatter_add(tgt, idx, src, axis=0):
    return tgt.clone().index_add_(axis, idx, src)
def _segment_sum(src, seg, n):
    out = torch.zeros((n,) + src.shape[1:], dtype=src.dtype)
    return out.index_add_(0, seg, src)
def _pad(x, pw):
    flat = tuple(v for pair in reversed(pw) for v in pair)
    return torch.nn.functional.pad(x, flat)

kernel_name = 'gated_mix__sliding_block__moe_arch_mod__prompt'

def setup_inputs():

    _w = (lambda a: (lambda t: t.to(torch.bfloat16) if t.is_floating_point() else t)(torch.as_tensor(a)))
    def _A_setup():
        B, S, D = 1, 2048, 640
        return {
            "x": _w((lambda *s: torch.randn(*s, dtype=torch.bfloat16) * 0.05)(B, S, D)),
            "wqkv": _w((lambda *s: torch.randn(*s, dtype=torch.bfloat16) * 1.6)(D, 3 * D)),
            "wo": _w((lambda *s: torch.randn(*s, dtype=torch.bfloat16) * 0.05)(D, D)),
        }
    _A_in = _A_setup()

    def _B_setup():
        B, S, D, F, C = 1, 2048, 640, 1536, 8
        return {
            "x": _w((lambda *s: torch.randn(*s, dtype=torch.bfloat16) * 0.05)(B, S, D)),
            "w_r": _w((lambda *s: torch.randn(*s, dtype=torch.bfloat16) * 3.2)(D)),
            "w_gate": _w((lambda *s: torch.randn(*s, dtype=torch.bfloat16) * 0.4)(D, F)),
            "w_up": _w((lambda *s: torch.randn(*s, dtype=torch.bfloat16) * 0.4)(D, F)),
            "w_down": _w((lambda *s: torch.randn(*s, dtype=torch.bfloat16) * 0.4)(F, D)),
        }
    _B_in = _B_setup()

    return {
        "A_x": _A_in["x"],
        "A_wqkv": _A_in["wqkv"],
        "A_wo": _A_in["wo"],
        "B_x": _B_in["x"],
        "B_w_r": _B_in["w_r"],
        "B_w_gate": _B_in["w_gate"],
        "B_w_up": _B_in["w_up"],
        "B_w_down": _B_in["w_down"],
        "gate_w": _w((lambda *s: torch.randn(*s, dtype=torch.bfloat16) * 0.05)(640) * 16.0),
    }

def reference(A_x, A_wqkv, A_wo, B_x, B_w_r, B_w_gate, B_w_up, B_w_down, gate_w):
    def _A_ref(x, wqkv, wo):
        B, S, D = x.shape
        H = 32; Dh = D // H; W = 256
        qkv = (lambda x, *d: x.permute(*d))((x @ wqkv).reshape(B, S, 3, H, Dh), 2, 0, 3, 1, 4)
        q, k, v = qkv[0], qkv[1], qkv[2]
        scores = torch.einsum("bhqd,bhkd->bhqk", q, k) * (Dh ** -0.5)
        idx = torch.arange(S)
        mask = (idx[:, None] - idx[None, :] >= 0) & (idx[:, None] - idx[None, :] < W)
        scores = torch.where(mask[None, None], scores, -1e9)
        p = _softmax(scores, axis=-1)
        o = torch.einsum("bhqk,bhkd->bhqd", p, v)
        return (lambda x, *d: x.permute(*d))(o, 0, 2, 1, 3).reshape(B, S, D) @ wo

    def _B_ref(x, w_r, w_gate, w_up, w_down):
        B, S, D = x.shape
        C = 8
        r = x @ w_r
        idx = torch.argsort(-r, axis=-1)[:, :C]
        idx_e = (lambda x, n, axis: torch.repeat_interleave(x, n, dim=axis))(idx.reshape(B, C, 1), D, axis=2)
        x_sel = (lambda x, i, axis: torch.take_along_dim(x, i, dim=axis))(x, idx_e, axis=1)
        g = _silu(x_sel @ w_gate)
        u = x_sel @ w_up
        y = (g * u) @ w_down
        r_sel = _sigmoid((lambda x, i, axis: torch.take_along_dim(x, i, dim=axis))(r, idx, axis=1))
        y = y * r_sel.reshape(B, C, 1)
        b_off = (torch.arange(B) * S).reshape(B, 1)
        flat_idx = (idx + b_off).reshape(B * C)
        zero = (x * 0.0).reshape(B * S, D)
        upd = _scatter_add(zero, flat_idx, y.reshape(B * C, D), axis=0)
        return x + upd.reshape(B, S, D)

    x = A_x
    g = _sigmoid((x * gate_w).sum(-1, keepdims=True))
    a = _A_ref(x, A_wqkv, A_wo)
    b = _B_ref(x, B_w_r, B_w_gate, B_w_up, B_w_down)
    return g * a + (1 - g) * b
```

```cpp
#include <hip/hip_runtime.h>
#include <hip/hip_bf16.h>
typedef __hip_bfloat16 bf16;
#define S 2048
#define D 640
#define F 1536
__device__ inline float b2f(bf16 x){return __bfloat162float(x);}
__device__ inline float rb(float x){return __bfloat162float(__float2bfloat16(x));}
__global__ void k_gemm(const bf16* A,const bf16* B,bf16* C,int M,int N,int K){
  int n=blockIdx.x*64+threadIdx.x%64, m=blockIdx.y*4+threadIdx.x/64;
  if(n>=N||m>=M)return; float acc=0; for(int k=0;k<K;k++)acc+=b2f(A[m*K+k])*b2f(B[k*N+n]);
  C[m*N+n]=__float2bfloat16(acc);
}
__global__ void k_attn(const bf16* qkv,bf16* o){
  int h=blockIdx.y; int q=blockIdx.x*64+threadIdx.x;
  float qv[20]; for(int d=0;d<20;d++)qv[d]=b2f(qkv[q*1920+h*20+d]);
  float m=-1e30f; int k0=q-255<0?0:q-255;
  for(int k=k0;k<=q;k++){float s=0;for(int d=0;d<20;d++)s+=qv[d]*b2f(qkv[k*1920+640+h*20+d]); s=s*0.22360679f; m=fmaxf(m,s);}
  float l=0,acc[20]; for(int d=0;d<20;d++)acc[d]=0;
  for(int k=k0;k<=q;k++){float s=0;for(int d=0;d<20;d++)s+=qv[d]*b2f(qkv[k*1920+640+h*20+d]); s=s*0.22360679f; float p=__expf(s-m); l+=p; for(int d=0;d<20;d++)acc[d]+=p*b2f(qkv[k*1920+1280+h*20+d]);}
  for(int d=0;d<20;d++)o[q*640+h*20+d]=__float2bfloat16(acc[d]/l);
}
__global__ void k_rg(const bf16* x,const bf16* wr,const bf16* gw,float* r,float* g){
  int t=blockIdx.x*64+threadIdx.x; float a=0,b=0; for(int d=0;d<D;d++){float xv=b2f(x[t*D+d]);a+=xv*b2f(wr[d]);b+=rb(xv*b2f(gw[d]));}
  r[t]=rb(a); g[t]=rb(1.f/(1.f+__expf(-rb(b))));
}
__global__ void k_top(const float* r,int* idx){
  int t=blockIdx.x*64+threadIdx.x; float v=r[t]; int rank=0; for(int j=0;j<S;j++){float u=r[j]; rank+=(u>v)||(u==v&&j<t);} if(rank<8)idx[rank]=t;
}
__global__ void k_h(const bf16* x,const int* idx,const bf16* wg,const bf16* wu,float* h){
  int f=blockIdx.x*64+threadIdx.x,c=blockIdx.y; int t=idx[c]; float a=0,b=0; for(int d=0;d<D;d++){float xv=b2f(x[t*D+d]);a+=xv*b2f(wg[d*F+f]);b+=xv*b2f(wu[d*F+f]);}
  a=rb(a);b=rb(b); h[c*F+f]=rb(rb(a/(1.f+__expf(-a)))*b);
}
__global__ void k_y(const float* h,const int* idx,const float* r,const bf16* wd,float* y){
  int n=blockIdx.x*64+threadIdx.x,c=blockIdx.y; float a=0; for(int f=0;f<F;f++)a+=h[c*F+f]*b2f(wd[f*D+n]);
  y[c*D+n]=rb(rb(a)*rb(1.f/(1.f+__expf(-r[idx[c]]))));
}
__global__ void k_fin(const bf16* x,const bf16* a,const float* g,const int* idx,const float* y,bf16* out){
  int n=blockIdx.x*64+threadIdx.x,t=blockIdx.y; float b=b2f(x[t*D+n]); for(int c=0;c<8;c++)if(idx[c]==t)b=rb(b+y[c*D+n]);
  float gg=g[t]; out[t*D+n]=__float2bfloat16(rb(gg*b2f(a[t*D+n]))+rb(rb(1.f-gg)*b));
}
extern "C" void kernel_launch(void* const* d_in,const int* in_sizes,int n_in,void* d_out,int out_size,void* d_ws,size_t ws_size,hipStream_t stream){
  const bf16* x=(const bf16*)d_in[0];const bf16* wqkv=(const bf16*)d_in[1];const bf16* wo=(const bf16*)d_in[2];
  const bf16* wr=(const bf16*)d_in[4];const bf16* wg=(const bf16*)d_in[5];const bf16* wu=(const bf16*)d_in[6];const bf16* wd=(const bf16*)d_in[7];const bf16* gw=(const bf16*)d_in[8];
  char* ws=(char*)d_ws; bf16* qkv=(bf16*)ws; bf16* o=(bf16*)(ws+(8<<20)); bf16* a=(bf16*)(ws+(12<<20)); float* r=(float*)(ws+(16<<20)); float* g=r+S; int* idx=(int*)(g+S); float* h=(float*)(idx+64); float* y=h+8*F;
  k_gemm<<<dim3(30,512),256,0,stream>>>(x,wqkv,qkv,S,1920,D);
  k_attn<<<dim3(32,32),64,0,stream>>>(qkv,o);
  k_gemm<<<dim3(10,512),256,0,stream>>>(o,wo,a,S,D,D);
  k_rg<<<32,64,0,stream>>>(x,wr,gw,r,g);
  k_top<<<32,64,0,stream>>>(r,idx);
  k_h<<<dim3(24,8),64,0,stream>>>(x,idx,wg,wu,h);
  k_y<<<dim3(10,8),64,0,stream>>>(h,idx,r,wd,y);
  k_fin<<<dim3(10,S),64,0,stream>>>(x,a,g,idx,y,(bf16*)d_out);
}
```

```cpp
#include <hip/hip_runtime.h>
#include <hip/hip_bf16.h>

typedef unsigned u32;
typedef unsigned long long u64;
typedef __attribute__((ext_vector_type(2))) unsigned u32x2;
typedef __attribute__((ext_vector_type(4))) unsigned u32x4;
typedef __attribute__((ext_vector_type(2))) float f32x2;
typedef __attribute__((ext_vector_type(4))) float f32x4;
typedef __attribute__((ext_vector_type(16))) float f32x16;
typedef __attribute__((ext_vector_type(2))) __bf16 bf16x2;
typedef __attribute__((ext_vector_type(8))) __bf16 bf16x8;
typedef __attribute__((ext_vector_type(4))) short s16x4;
#define LAS __attribute__((address_space(3)))
#define DI __device__ __forceinline__

constexpr u32 WS_EPOCH = 0;
constexpr u32 WS_RG    = 4096;
constexpr u32 WS_SEL   = 20480;
constexpr u32 WS_FLY   = 24576;
constexpr u32 WS_FL1   = 32768;
constexpr u32 WS_FL2   = 65536;
constexpr u32 WS_YP    = 262144;
constexpr u32 WS_Q     = 1u << 20;
constexpr u32 SZ_T     = 2621440;
constexpr u32 WS_K     = WS_Q + SZ_T;
constexpr u32 WS_V     = WS_K + SZ_T;
constexpr u32 WS_O     = WS_V + SZ_T;
constexpr u32 L_XS   = 131072;
constexpr u32 L_H    = L_XS + 10368;
constexpr u32 L_CAND = L_H + 2176;
constexpr u32 L_MISC = L_CAND + 8192;
constexpr u32 L_K0   = 102400;
constexpr u32 L_K1   = L_K0 + 16384;
constexpr u32 L_V    = L_K1 + 4096;
constexpr u32 L_RED  = L_K0;
constexpr u32 L_YB   = 145408;
constexpr u32 L_MAP  = L_YB + 2560;
constexpr u32 L_YT   = L_MAP + 256;
constexpr u32 LDS_BYTES = 160 * 1024;
constexpr int SPIN_MAX = 1 << 17;

struct Params {
    const char *x, *wqkv, *wo, *wr, *wg, *wu, *wd, *gw;
    char *out, *ws;
};

template <class T> DI T lds_ld(u32 a) { return *(const LAS T*)(a); }
template <class T> DI void lds_st(u32 a, T v) { *(LAS T*)(a) = v; }
DI s16x4 lds_tr(u32 a) { return __builtin_amdgcn_ds_read_tr16_b64_v4i16((LAS s16x4*)a); }
DI bf16x8 cat8(s16x4 a, s16x4 b) {
    u32x2 x = __builtin_bit_cast(u32x2, a), y = __builtin_bit_cast(u32x2, b);
    u32x4 r = {x[0], x[1], y[0], y[1]};
    return __builtin_bit_cast(bf16x8, r);
}
DI u32 pk2(float a, float b) {
    f32x2 v = {a, b};
    return __builtin_bit_cast(u32, __builtin_convertvector(v, bf16x2));
}
DI float u2f(u32 v) { return __builtin_bit_cast(float, v); }
DI u32 f2u(float v) { return __builtin_bit_cast(u32, v); }
DI float bflo(u32 v) { return __builtin_bit_cast(float, v << 16); }
DI float bfhi(u32 v) { return __builtin_bit_cast(float, v & 0xffff0000u); }
DI float rbf(float a) { return bflo(pk2(a, a)); }
DI float sigm(float a) { return __builtin_amdgcn_rcpf(1.0f + __builtin_amdgcn_exp2f(-1.44269504f * a)); }
DI u32 uni(u32 v) { return __builtin_amdgcn_readfirstlane(v); }

DI void dma16(u32 voff, const char* base, u32 dst) {
    asm volatile("s_mov_b32 m0, %2\n\ts_nop 0\n\tglobal_load_lds_dwordx4 %0, %1"
                 :: "v"(voff), "s"(base), "s"(dst) : "memory");
}
template <int OFF> DI u32x4 ld4(u32 voff, const char* base) {
    u32x4 d; asm volatile("global_load_dwordx4 %0, %1, %2 offset:%3" : "=v"(d) : "v"(voff), "s"(base), "i"(OFF) : "memory"); return d; }
template <int OFF> DI u32 ld1(u32 voff, const char* base) {
    u32 d; asm volatile("global_load_dword %0, %1, %2 offset:%3" : "=v"(d) : "v"(voff), "s"(base), "i"(OFF) : "memory"); return d; }
template <int OFF> DI u32x2 ld2(u32 voff, const char* base) {
    u32x2 d; asm volatile("global_load_dwordx2 %0, %1, %2 offset:%3" : "=v"(d) : "v"(voff), "s"(base), "i"(OFF) : "memory"); return d; }
template <int OFF> DI u32x4 ld4c(u32 voff, const char* base) {
    u32x4 d; asm volatile("global_load_dwordx4 %0, %1, %2 offset:%3 sc1" : "=v"(d) : "v"(voff), "s"(base), "i"(OFF) : "memory"); return d; }
template <int OFF> DI u32x2 ld2c(u32 voff, const char* base) {
    u32x2 d; asm volatile("global_load_dwordx2 %0, %1, %2 offset:%3 sc1" : "=v"(d) : "v"(voff), "s"(base), "i"(OFF) : "memory"); return d; }
template <int OFF> DI u32 ld1c(u32 voff, const char* base) {
    u32 d; asm volatile("global_load_dword %0, %1, %2 offset:%3 sc1" : "=v"(d) : "v"(voff), "s"(base), "i"(OFF) : "memory"); return d; }
template <int OFF> DI void st4c(u32 voff, u32x4 v, char* base) {
    asm volatile("global_store_dwordx4 %0, %1, %2 offset:%3 sc1\n\ts_nop 1" :: "v"(voff), "v"(v), "s"(base), "i"(OFF) : "memory"); }
template <int OFF> DI void st2c(u32 voff, u32x2 v, char* base) {
    asm volatile("global_store_dwordx2 %0, %1, %2 offset:%3 sc1" :: "v"(voff), "v"(v), "s"(base), "i"(OFF) : "memory"); }
template <int OFF> DI void st1c(u32 voff, u32 v, char* base) {
    asm volatile("global_store_dword %0, %1, %2 offset:%3 sc1" :: "v"(voff), "v"(v), "s"(base), "i"(OFF) : "memory"); }
#define WAITVM(n) asm volatile("s_waitcnt vmcnt(" #n ")" ::: "memory")
#define BAR() asm volatile("s_waitcnt lgkmcnt(0)\n\ts_barrier" ::: "memory")
#define PIN(x) asm volatile("" : "+v"(x))

DI void poll_flags(u32 voff, const char* base, u32 tag, bool need) {
#pragma unroll 1
    for (int it = 0; it < SPIN_MAX; ++it) {
        u32 v;
        asm volatile("global_load_dword %0, %1, %2 sc1\n\ts_waitcnt vmcnt(0)" : "=v"(v) : "v"(voff), "s"(base) : "memory");
        if (__builtin_amdgcn_ballot_w64(need && v != tag) == 0) break;
        __builtin_amdgcn_s_sleep(2);
    }
}
DI u32 poll_granule(u32 voff, const char* base, u32 tag) {
    u32x2 v;
#pragma unroll 1
    for (int it = 0; it < SPIN_MAX; ++it) {
        asm volatile("global_load_dwordx2 %0, %1, %2 sc1\n\ts_waitcnt vmcnt(0)" : "=v"(v) : "v"(voff), "s"(base) : "memory");
        if (__builtin_amdgcn_ballot_w64(v[1] != tag) == 0) break;
        __builtin_amdgcn_s_sleep(2);
    }
    return v[0];
}
template <int CTRL> DI float dpp_add(float v) {
    return v + __builtin_bit_cast(float, __builtin_amdgcn_update_dpp(0, __builtin_bit_cast(int, v), CTRL, 0xf, 0xf, true));
}
template <int CTRL> DI u32 dpp_max(u32 v) {
    u32 o = (u32)__builtin_amdgcn_update_dpp(0, (int)v, CTRL, 0xf, 0xf, true);
    return v > o ? v : o;
}
#define STAMP(i)

struct Ring {
    const char *a, *b;
    u32 va0, va1, vb0, vb1;
};
DI void ring_issue(const Ring& r, u32 slot, u32 w) {
    u32 d = slot * 32768 + w * 2048;
    dma16(r.va0, r.a, d);
    dma16(r.va1, r.a, d + 1024);
    dma16(r.vb0, r.b, d + 16384);
    dma16(r.vb1, r.b, d + 17408);
}

template <bool SWAP>
DI void gemm_loop(Ring& r, u32 w, u32 l, f32x16& c0, f32x16& c1) {
    u32 tw = w >> 1, cw = w & 1, h = l >> 5, row = tw * 32 + (l & 31);
    u32 xa = row * 128 + ((h ^ ((row >> 1) & 7)) << 4);
    u32 q = (l >> 2) & 3, pp = l & 3, g1 = (l >> 4) & 1;
    u32 ch = SWAP ? (pp >> 1) : (pp & 1), hf = SWAP ? (pp & 1) : (pp >> 1);
    u32 wa = 16384 + (8 * h + q) * 256 + (((8 * cw + 2 * g1 + ch) ^ (q << 2)) << 4) + hf * 8;
    u32 x0 = xa, x1 = xa ^ 32, x2 = xa ^ 64, x3 = xa ^ 96, w0 = wa, w1 = wa ^ 64;
#pragma unroll 1
    for (int s = 0; s < 10; ++s) {
        if (s < 8) WAITVM(8); else if (s == 8) WAITVM(4); else WAITVM(0);
        BAR();
        if (s < 7) { r.a += 128; r.b += 64 * 3840; ring_issue(r, (s + 3) & 3, w); }
#pragma unroll
        for (int k = 0; k < 4; ++k) {
            bf16x8 xf = lds_ld<bf16x8>(k == 0 ? x0 : k == 1 ? x1 : k == 2 ? x2 : x3);
            bf16x8 a0 = cat8(lds_tr(w0 + k * 4096), lds_tr(w0 + k * 4096 + 1024));
            bf16x8 a1 = cat8(lds_tr(w1 + k * 4096), lds_tr(w1 + k * 4096 + 1024));
            if (SWAP) {
                c0 = __builtin_amdgcn_mfma_f32_32x32x16_bf16(xf, a0, c0, 0, 0, 0);
                c1 = __builtin_amdgcn_mfma_f32_32x32x16_bf16(xf, a1, c1, 0, 0, 0);
            } else {
                c0 = __builtin_amdgcn_mfma_f32_32x32x16_bf16(a0, xf, c0, 0, 0, 0);
                c1 = __builtin_amdgcn_mfma_f32_32x32x16_bf16(a1, xf, c1, 0, 0, 0);
            }
        }
        u32 dl = (u32)((s ^ (s + 1)) & 3) << 15;
        x0 ^= dl; x1 ^= dl; x2 ^= dl; x3 ^= dl; w0 ^= dl; w1 ^= dl;
    }
}
DI u32x4 pk8(const f32x16& c, int o) {
    u32x4 r = {pk2(c[o], c[o + 1]), pk2(c[o + 2], c[o + 3]), pk2(c[o + 4], c[o + 5]), pk2(c[o + 6], c[o + 7])};
    return r;
}
DI f32x16 qk_tile(u32 ka, u32 kb, bf16x8 q0, s16x4 q1, const f32x16& c) {
    f32x16 s = __builtin_amdgcn_mfma_f32_32x32x16_bf16(lds_ld<bf16x8>(ka), q0, c, 0, 0, 0);
    return __builtin_amdgcn_mfma_f32_32x32x8bf16_1k(lds_ld<s16x4>(kb), q1, s, 0, 0, 0);
}
DI f32x16 pv_tile(u32 va, f32x16 s, const f32x16& o) {
#pragma unroll
    for (int i = 0; i < 16; ++i) s[i] = __builtin_amdgcn_exp2f(s[i]);
    f32x16 r = __builtin_amdgcn_mfma_f32_32x32x16_bf16(lds_ld<bf16x8>(va), __builtin_bit_cast(bf16x8, pk8(s, 0)), o, 0, 0, 0);
    return __builtin_amdgcn_mfma_f32_32x32x16_bf16(lds_ld<bf16x8>(va + 672), __builtin_bit_cast(bf16x8, pk8(s, 8)), r, 0, 0, 0);
}

__global__ void __launch_bounds__(512, 2) fused_kernel(Params p) {
    const u32 b = blockIdx.x, tid = threadIdx.x, l = tid & 63, w = uni(tid >> 6);
    const u32 xcd = b & 7, rk = b >> 3;
    STAMP(0);
    u32 ep = ld1c<0>(0u, p.ws);
    const char* xrow = p.x + (size_t)(b * 8 + w) * 1280;
    u32 l20 = l * 20;
    u32x4 rx4 = ld4<0>(l20, xrow), rw4 = ld4<0>(l20, p.wr), rg4 = ld4<0>(l20, p.gw);
    u32 rx1 = ld1<16>(l20, xrow), rw1 = ld1<16>(l20, p.wr), rg1 = ld1<16>(l20, p.gw);

    const bool isg = rk < 30;
    const u32 t1 = xcd * 2 + (rk >= 15), j1 = rk >= 15 ? rk - 15 : rk;
    const u32 fi = xcd * 2 + (rk - 30);
    Ring r;
    {
        u32 wr_ = w * 8 + (l >> 4), wc = ((l & 15) ^ ((l >> 4) << 2)) << 4;
        if (isg) {
            u32 row = w * 16 + (l >> 3), f = (row >> 1) & 7;
            r.va0 = row * 1280 + (((l & 7) ^ f) << 4);
            r.va1 = (row + 8) * 1280 + (((l & 7) ^ f ^ 4) << 4);
            r.vb0 = wr_ * 3840 + wc;
            r.vb1 = r.vb0 + 4 * 3840;
            r.a = p.x + (size_t)t1 * 128 * 1280;
            r.b = p.wqkv + j1 * 256;
        } else {
            r.va0 = r.vb0 = wr_ * 3072 + wc;
            r.va1 = r.vb1 = r.va0 + 4 * 3072;
            u32 fo = (fi < 12 ? fi : 0) * 256;
            r.a = p.wg + fo;
            r.b = p.wu + fo;
        }
    }
    const u32 sa = isg ? 128 : 64 * 3072, sb = isg ? 64 * 3840 : 64 * 3072;
    ring_issue(r, 0, w); r.a += sa; r.b += sb;
    ring_issue(r, 1, w); r.a += sa; r.b += sb;
    ring_issue(r, 2, w);
    asm volatile("s_waitcnt vmcnt(12)" : "+v"(ep), "+v"(rx4), "+v"(rw4), "+v"(rg4), "+v"(rx1), "+v"(rw1), "+v"(rg1) :: "memory");
    const u32 tag = uni(ep) + 1;
    {
        float ar = 0.f, ag = 0.f;
#pragma unroll
        for (int i = 0; i < 5; ++i) {
            bf16x2 xv = __builtin_bit_cast(bf16x2, i < 4 ? rx4[i & 3] : rx1);
            ar = __builtin_amdgcn_fdot2_f32_bf16(xv, __builtin_bit_cast(bf16x2, i < 4 ? rw4[i & 3] : rw1), ar, false);
            ag = __builtin_amdgcn_fdot2_f32_bf16(xv, __builtin_bit_cast(bf16x2, i < 4 ? rg4[i & 3] : rg1), ag, false);
        }
        auto sw = __builtin_amdgcn_permlane32_swap(f2u(ar), f2u(ag), false, false);
        float v = u2f(sw[0]) + u2f(sw[1]);
        v = dpp_add<0x111>(v); v = dpp_add<0x112>(v); v = dpp_add<0x114>(v); v = dpp_add<0x118>(v);
        v = dpp_add<0x142>(v);
        float rs = __builtin_bit_cast(float, __builtin_amdgcn_readlane(__builtin_bit_cast(int, v), 31));
        float gs = __builtin_bit_cast(float, __builtin_amdgcn_readlane(__builtin_bit_cast(int, v), 63));
        u32x2 gr = {pk2(sigm(rbf(gs)), rs), tag};
        if (l == 0) st2c<0>((b * 8 + w) * 8, gr, p.ws + WS_RG);
    }
    STAMP(1);

    if (isg) {
        f32x16 c0 = {}, c1 = {};
        const u32 kind = j1 / 5, jc = j1 - kind * 5, tile = t1 * 4 + (w >> 1);
        if (kind == 2) {
            gemm_loop<true>(r, w, l, c0, c1);
            STAMP(2);
            u32 col = jc * 128 + (w & 1) * 64 + (l & 31), h = l >> 5;
            u32 hd0 = (col * 3277) >> 16, hd1 = ((col + 32) * 3277) >> 16;
            u32 v0 = hd0 * (64 * 1280) + h * 320 + (col - hd0 * 20) * 16;
            u32 v1 = hd1 * (64 * 1280) + h * 320 + (col + 32 - hd1 * 20) * 16;
            char* base = p.ws + WS_V + tile * 1280;
            st4c<0>(v0, pk8(c0, 0), base); st4c<640>(v0, pk8(c0, 8), base);
            st4c<0>(v1, pk8(c1, 0), base); st4c<640>(v1, pk8(c1, 8), base);
        } else {
            gemm_loop<false>(r, w, l, c0, c1);
            STAMP(2);
            if (kind == 0) { c0 *= 0.32259784f; c1 *= 0.32259784f; }
            char* base = p.ws + WS_Q + kind * SZ_T + (tile * 80 + jc * 16 + (w & 1) * 8) * 512;
            u32 vo = l * 16;
            st4c<0>(vo, pk8(c0, 0), base); st4c<1024>(vo, pk8(c0, 8), base);
            st4c<2048>(vo, pk8(c1, 0), base); st4c<3072>(vo, pk8(c1, 8), base);
        }
        WAITVM(0);
        BAR();
        if (tid == 0) st1c<0>((t1 * 16 + j1) * 128, tag, p.ws + WS_FL1);
        STAMP(3);
    } else if (fi < 12) {
        u32 key[4];
        {
            u32x2 g0, g1, g2, g3;
            const char* gb = p.ws + WS_RG;
#pragma unroll 1
            for (int it = 0; it < SPIN_MAX; ++it) {
                g0 = ld2c<0>(tid * 8, gb); g1 = ld2c<0>(tid * 8, gb + 4096);
                g2 = ld2c<0>(tid * 8, gb + 8192); g3 = ld2c<0>(tid * 8, gb + 12288);
                asm volatile("s_waitcnt vmcnt(0)" : "+v"(g0), "+v"(g1), "+v"(g2), "+v"(g3) :: "memory");
                if (__builtin_amdgcn_ballot_w64(g0[1] != tag || g1[1] != tag || g2[1] != tag || g3[1] != tag) == 0) break;
                __builtin_amdgcn_s_sleep(2);
            }
#pragma unroll
            for (int i = 0; i < 4; ++i) {
                u32 v = (i == 0 ? g0[0] : i == 1 ? g1[0] : i == 2 ? g2[0] : g3[0]) >> 16;
                v ^= (v & 0x8000u) ? 0xffffu : 0x8000u;
                key[i] = (v << 16) | (2047 - (i * 512 + tid));
            }
        }
        u32 m = max(max(key[0], key[1]), max(key[2], key[3]));
        m = dpp_max<0x111>(m); m = dpp_max<0x112>(m); m = dpp_max<0x114>(m); m = dpp_max<0x118>(m);
        m = dpp_max<0x142>(m); m = dpp_max<0x143>(m);
        if (l == 63) lds_st<u32>(L_MISC + w * 4, m);
        if (tid == 0) lds_st<u32>(L_MISC + 32, 0u);
        BAR();
        if (fi == 0 && tid == 0) st1c<0>(0u, tag, p.ws + WS_EPOCH);
        u32x4 ma = lds_ld<u32x4>(L_MISC), mb = lds_ld<u32x4>(L_MISC + 16);
        u32 thr = min(min(min(ma[0], ma[1]), min(ma[2], ma[3])), min(min(mb[0], mb[1]), min(mb[2], mb[3])));
#pragma unroll
        for (int i = 0; i < 4; ++i)
            if (key[i] >= thr) {
                u32 n = __hip_atomic_fetch_add((LAS u32*)(L_MISC + 32), 1u, __ATOMIC_RELAXED, __HIP_MEMORY_SCOPE_WORKGROUP);
                lds_st<u32>(L_CAND + n * 4, key[i]);
            }
        BAR();
        u32 n = lds_ld<u32>(L_MISC + 32);
#pragma unroll 1
        for (u32 i = tid; i < n; i += 512) {
            u32 k = lds_ld<u32>(L_CAND + i * 4), rank = 0;
#pragma unroll 4
            for (u32 jx = 0; jx < n; ++jx) rank += lds_ld<u32>(L_CAND + jx * 4) > k;
            if (rank < 8) lds_st<u32>(L_MISC + 48 + rank * 4, k);
        }
        BAR();
        {
            u32 k = uni(lds_ld<u32>(L_MISC + 48 + w * 4));
            if (fi == 0 && l == 0) { u32x2 g = {k, tag}; st2c<0>(w * 8, g, p.ws + WS_SEL); }
            const char* xs = p.x + (size_t)(2047 - (k & 0xffffu)) * 1280;
            dma16(l * 16, xs, L_XS + w * 1296);
            if (l < 16) dma16(l * 16, xs + 1024, L_XS + w * 1296 + 1024);
        }
        WAITVM(0);
        STAMP(2);
        const u32 kg = l >> 4, q = (l >> 2) & 3, pp = l & 3;
        u32 wa = (8 * kg + q) * 256 + (((2 * w + (pp >> 1)) ^ (q << 2)) << 4) + (pp & 1) * 8;
        u32 xa = L_XS + (l & 7) * 1296 + kg * 16;
        f32x4 cg = {}, cu = {};
#pragma unroll 1
        for (int s = 0; s < 15; ++s) {
            if (s < 13) WAITVM(8); else if (s == 13) WAITVM(4); else WAITVM(0);
            BAR();
            if (s < 12) {
                if (s == 7) {
                    u32 wr_ = w * 8 + (l >> 4), wc = ((l & 15) ^ ((l >> 4) << 2)) << 4;
                    r.va0 = r.vb0 = wr_ * 1280 + wc;
                    r.va1 = r.vb1 = r.va0 + 4 * 1280;
                    r.a = p.wd + (size_t)fi * 128 * 1280;
                    r.b = r.a + 64 * 1280;
                } else {
                    u32 st = s < 7 ? 64 * 3072 : 256;
                    r.a += st; r.b += st;
                }
                ring_issue(r, (s + 3) & 3, w);
            }
            u32 a = wa + (s & 3) * 32768;
            if (s < 10) {
#pragma unroll
                for (int k = 0; k < 2; ++k) {
                    bf16x8 xf = lds_ld<bf16x8>(xa + k * 64);
                    cg = __builtin_amdgcn_mfma_f32_16x16x32_bf16(cat8(lds_tr(a + k * 8192), lds_tr(a + k * 8192 + 1024)), xf, cg, 0, 0, 0);
                    cu = __builtin_amdgcn_mfma_f32_16x16x32_bf16(cat8(lds_tr(a + k * 8192 + 16384), lds_tr(a + k * 8192 + 17408)), xf, cu, 0, 0, 0);
                }
                xa += 128;
                if (s == 9) {
                    float hv[4];
#pragma unroll
                    for (int i = 0; i < 4; ++i) { float g = rbf(cg[i]); hv[i] = rbf(g * sigm(g)) * rbf(cu[i]); }
                    u32x2 hp = {pk2(hv[0], hv[1]), pk2(hv[2], hv[3])};
                    if ((l & 15) < 8) lds_st<u32x2>(L_H + (l & 7) * 272 + w * 32 + kg * 8, hp);
                }
            } else {
                f32x4 y = {};
#pragma unroll
                for (int k = 0; k < 4; ++k) {
                    bf16x8 hf = lds_ld<bf16x8>(L_H + (l & 7) * 272 + k * 64 + kg * 16);
                    y = __builtin_amdgcn_mfma_f32_16x16x32_bf16(cat8(lds_tr(a + k * 8192), lds_tr(a + k * 8192 + 1024)), hf, y, 0, 0, 0);
                }
                u32x4 yv = __builtin_bit_cast(u32x4, y);
                asm volatile("s_nop 8" : "+v"(yv));
                if ((l & 15) < 8)
                    st4c<0>((l & 7) * 2560 + w * 64 + kg * 16, yv, p.ws + WS_YP + fi * 20480 + (s - 10) * 512);
            }
        }
        WAITVM(0);
        BAR();
        if (tid == 0) st1c<0>(fi * 128, tag, p.ws + WS_FLY);
        STAMP(3);
    } else {
        WAITVM(0);
        BAR();
    }

    const u32 r2 = (rk + 2) & 31;
    const u32 u = r2 >> 2, head = xcd * 4 + (r2 & 3);
    const u32 tt = xcd * 4 + (rk >> 3), cgp = rk & 7;
    {
        const u32 odd = head & 1, c5 = (head >> 1) * 5, h = l >> 5, tok = l & 31;
        const bool have = (u > 0) | (w >= 4);
        {
            u32 kind = l >> 1, cc = c5 + odd * 2 + (l & 1) * 2;
            u32 tq = kind == 0 ? 2 * u + (w >> 2) : 2 * u - 2 + (w >> 1);
            bool need = l < 6 && (kind == 0 || have);
            poll_flags(need ? (tq * 16 + kind * 5 + (cc >> 4)) * 128 : 0u, p.ws + WS_FL1, tag, need);
        }
        STAMP(4);
        const u32 k0 = have ? u * 8 - 8 + 2 * w : 0;
        const char* qb = p.ws + WS_Q + ((u * 8 + w) * 80 + c5) * 512;
        const char* kb = p.ws + WS_K + (k0 * 80 + c5) * 512;
        const char* vb = p.ws + WS_V + (head * 64 + k0) * 1280;
        u32 vm = l * 16 + odd * 1536, vs = 1024 + tok * 16 + odd * 8;
        u32x4 qm = ld4c<0>(vm, qb);
        u32x2 qs = ld2c<0>(vs, qb);
        u32x4 km0 = {}, km1 = {}, v0 = {}, v1 = {}, v2 = {};
        u32x2 ks = {};
        if (have) {
            km0 = ld4c<0>(vm, kb); km1 = ld4c<0>(vm + 40960, kb);
            ks = ld2c<0>(vs + h * 40960, kb);
            v0 = ld4c<0>(l * 16, vb); v1 = ld4c<1024>(l * 16, vb); v2 = ld4c<2048>(tok * 16, vb);
        }
        {
            u32 rr = (l * 26) >> 8;
            u32 vo = rr * 1280 + (l - rr * 10) * 16;
            const char* wb = p.wo + w * 80 * 1280 + cgp * 160;
            if (l < 60) {
#pragma unroll 1
                for (int n = 0; n < 14; ++n) {
                    u32 r0 = n < 13 ? n * 6 : 74;
                    dma16(vo, wb + r0 * 1280, (w * 80 + r0) * 160);
                }
            }
        }
        f32x16 cd, ce;
#pragma unroll
        for (int i = 0; i < 16; ++i) {
            bool le = (u32)((i & 3) + 8 * (i >> 2)) + 4 * h <= tok;
            cd[i] = le ? 0.f : -1e30f;
            ce[i] = le ? -1e30f : 0.f;
        }
        u32 p1 = l + 64, p2 = tok + 128;
        u32 d0 = (l * 3277) >> 16, d1 = (p1 * 3277) >> 16, d2 = (p2 * 3277) >> 16;
        u32 lv = L_V + w * 2688;
        asm volatile("s_waitcnt vmcnt(14)" : "+v"(qm), "+v"(qs), "+v"(km0), "+v"(km1), "+v"(ks), "+v"(v0), "+v"(v1), "+v"(v2) :: "memory");
        lds_st<u32x4>(L_K0 + w * 2048 + l * 16, km0);
        lds_st<u32x4>(L_K0 + w * 2048 + 1024 + l * 16, km1);
        lds_st<u32x2>(L_K1 + w * 512 + l * 8, ks);
        lds_st<u32x4>(lv + l * 16 + d0 * 16, v0);
        lds_st<u32x4>(lv + p1 * 16 + d1 * 16, v1);
        if (l < 32) lds_st<u32x4>(lv + p2 * 16 + d2 * 16, v2);
        else if (l < 40) { u32x4 one = {0x3f803f80u, 0x3f803f80u, 0x3f803f80u, 0x3f803f80u}; lds_st<u32x4>(lv + (l - 32) * 336 + 320, one); }
        if (h) { qs[0] = 0; qs[1] = 0; }
        BAR();
        STAMP(5);
        const bf16x8 q0 = __builtin_bit_cast(bf16x8, qm);
        const s16x4 q1 = __builtin_bit_cast(s16x4, qs);
        const u32 ka = L_K0 + w * 1024 + l * 16, kb1 = L_K1 + w * 256 + tok * 8, va = L_V + w * 1344 + h * 336 + tok * 16;
        const int j0 = u ? 0 : 8 - (int)w;
        f32x16 o = {};
        const f32x16 z = {};
        {
            f32x16 s = qk_tile(ka + 8 * 1024, kb1 + 8 * 256, q0, q1, cd);
#pragma unroll 1
            for (int j = 8; j > (j0 > 1 ? j0 : 1); --j) {
                f32x16 sn = qk_tile(ka + (j - 1) * 1024, kb1 + (j - 1) * 256, q0, q1, z);
                o = pv_tile(va + j * 1344, s, o);
                s = sn;
            }
            if (j0 == 0) {
                f32x16 sn = qk_tile(ka, kb1, q0, q1, ce);
                o = pv_tile(va + 1344, s, o);
                o = pv_tile(va, sn, o);
            } else {
                o = pv_tile(va + j0 * 1344, s, o);
            }
        }
        auto lsw = __builtin_amdgcn_permlane32_swap(f2u(o[8]), f2u(o[8]), false, false);
        float lsum = u2f(lsw[1]);
        if (__builtin_amdgcn_ballot_w64(!(lsum >= 0x1p-64f && lsum <= 0x1p64f)) != 0) {
            float m = -3e38f;
#pragma unroll 1
            for (int j = j0; j <= 8; ++j) {
                f32x16 s = qk_tile(ka + j * 1024, kb1 + j * 256, q0, q1, j == 8 ? cd : j == 0 ? ce : z);
#pragma unroll
                for (int i = 0; i < 16; ++i) m = fmaxf(m, s[i]);
            }
            auto msw = __builtin_amdgcn_permlane32_swap(f2u(m), f2u(m), false, false);
            m = fmaxf(u2f(msw[0]), u2f(msw[1]));
            o = z;
#pragma unroll 1
            for (int j = j0; j <= 8; ++j) {
                f32x16 s = qk_tile(ka + j * 1024, kb1 + j * 256, q0, q1, j == 8 ? cd : j == 0 ? ce : z);
                o = pv_tile(va + j * 1344, s - m, o);
            }
            lsw = __builtin_amdgcn_permlane32_swap(f2u(o[8]), f2u(o[8]), false, false);
            lsum = u2f(lsw[1]);
        }
        STAMP(6);
        float inv = __builtin_amdgcn_rcpf(lsum);
        o *= inv;
        char* ob = p.ws + WS_O + ((u * 8 + w) * 32 + head) * 1280;
        st4c<0>(l * 16, pk8(o, 0), ob);
        if (l < 32) { u32x2 sg = {pk2(o[8], o[9]), pk2(o[10], o[11])}; st2c<1024>(l * 8, sg, ob); }
        WAITVM(0);
        if (l == 0) st1c<0>(((u * 8 + w) * 32 + head) * 64, tag, p.ws + WS_FL2);
        STAMP(7);
    }

    {
        const u32 ts = w & 3, kh = w >> 2, kg = l >> 4, t16 = l & 15;
        const u32 tokl = ts * 16 + t16, token = tt * 64 + tokl;
        if (tid < 64) lds_st<int>(L_MAP + tid * 4, -1);
        const u32 gv = poll_granule(token * 8, p.ws + WS_RG, tag);
        const u32 sk = uni(poll_granule(w * 8, p.ws + WS_SEL, tag));
        const u32 xo = token * 1280 + cgp * 160 + kg * 8 + kh * 96;
        u32x2 xr0 = ld2<0>(xo, p.x), xr1 = ld2<32>(xo, p.x), xr2 = ld2<64>(kh ? xo - 64 : xo, p.x);
        BAR();
        const u32 st = 2047 - (sk & 0xffffu);
        if ((st >> 6) == tt) {
            poll_flags(l < 12 ? l * 128 : 0u, p.ws + WS_FLY, tag, l < 12);
            u32 p3 = (l * 13) >> 8, qd = l - p3 * 20;
            u32 vo = (p3 < 3 ? p3 : 0) * 20480 + w * 2560 + cgp * 320 + qd * 16;
            const char* yb = p.ws + WS_YP;
            u32x4 y0 = ld4c<0>(vo, yb), y1 = ld4c<0>(vo + 61440, yb), y2 = ld4c<0>(vo + 122880, yb), y3 = ld4c<0>(vo + 184320, yb);
            asm volatile("s_waitcnt vmcnt(0)" : "+v"(y0), "+v"(y1), "+v"(y2), "+v"(y3) :: "memory");
            f32x4 ys = (__builtin_bit_cast(f32x4, y0) + __builtin_bit_cast(f32x4, y1)) + (__builtin_bit_cast(f32x4, y2) + __builtin_bit_cast(f32x4, y3));
            lds_st<f32x4>(L_YT + w * 1024 + l * 16, ys);
            asm volatile("s_waitcnt lgkmcnt(0)" ::: "memory");
            if (l < 20) {
                ys += lds_ld<f32x4>(L_YT + w * 1024 + l * 16 + 320) + lds_ld<f32x4>(L_YT + w * 1024 + l * 16 + 640);
                u32 rb = sk >> 16;
                rb ^= (rb & 0x8000u) ? 0x8000u : 0xffffu;
                float rsg = rbf(sigm(bflo(rb)));
#pragma unroll
                for (int i = 0; i < 4; ++i) ys[i] = rbf(rbf(ys[i]) * rsg);
                lds_st<f32x4>(L_YB + w * 320 + l * 16, ys);
            }
            if (l == 0) lds_st<int>(L_MAP + (st & 63) * 4, (int)w);
        }
        const u32 tile = tt * 2 + (ts >> 1), t32 = (ts & 1) * 16 + t16;
        poll_flags((tile * 32 + kh * 16 + t16) * 64, p.ws + WS_FL2, tag, true);
        STAMP(8);
        const char* ob = p.ws + WS_O + (tile * 32 + kh * 16) * 1280;
        u32 vm = (kg >> 1) * 1280 + (kg & 1) * 512 + t32 * 16, vs = kg * 1280 + 1024 + t32 * 8;
        u32x4 om[8]; u32x2 os[4];
#pragma unroll
        for (int m = 0; m < 8; ++m) om[m] = ld4c<0>(vm, ob + m * 2560);
#pragma unroll
        for (int m = 0; m < 4; ++m) os[m] = ld2c<0>(vs, ob + m * 5120);
        const u32 q = (l >> 2) & 3, pp = l & 3;
        const u32 am = (kh * 320 + (kg >> 1) * 20 + (kg & 1) * 4 + q) * 160 + pp * 8;
        const u32 as = (kh * 320 + kg * 20 + 16 + q) * 160 + pp * 8;
        f32x4 acc[5] = {};
#define P3STEP(B, A0, A1)                                                                                   \
        _Pragma("unroll") for (int n = 0; n < 5; ++n)                                                       \
            acc[n] = __builtin_amdgcn_mfma_f32_16x16x32_bf16(cat8(lds_tr((A0) + n * 32), lds_tr((A1) + n * 32)), (B), acc[n], 0, 0, 0);
#define P3MAIN(m, cnt)                                                                                      \
        asm volatile("s_waitcnt vmcnt(" #cnt ")" : "+v"(om[m]) :: "memory");                                \
        P3STEP(__builtin_bit_cast(bf16x8, om[m]), am + m * 6400, am + m * 6400 + 1280)
        P3MAIN(0, 11) P3MAIN(1, 10) P3MAIN(2, 9) P3MAIN(3, 8) P3MAIN(4, 7) P3MAIN(5, 6) P3MAIN(6, 5) P3MAIN(7, 4)
        asm volatile("s_waitcnt vmcnt(0)" : "+v"(os[0]), "+v"(os[1]), "+v"(os[2]), "+v"(os[3]), "+v"(xr0), "+v"(xr1), "+v"(xr2) :: "memory");
        P3STEP(cat8(__builtin_bit_cast(s16x4, os[0]), __builtin_bit_cast(s16x4, os[1])), as, as + 12800)
        P3STEP(cat8(__builtin_bit_cast(s16x4, os[2]), __builtin_bit_cast(s16x4, os[3])), as + 25600, as + 38400)
        STAMP(9);
        const u32 ra = L_RED + ts * 5120 + l * 16;
        if (kh) {
            lds_st<f32x4>(ra, acc[0]); lds_st<f32x4>(ra + 1024, acc[1]); lds_st<f32x4>(ra + 2048, acc[2]);
            acc[0] = acc[3]; acc[1] = acc[4];
        } else {
            lds_st<f32x4>(ra + 3072, acc[3]); lds_st<f32x4>(ra + 4096, acc[4]);
        }
        BAR();
        const u32 rb = ra + kh * 3072;
        const float g = bflo(gv), og = rbf(1.0f - g);
        const int slot = lds_ld<int>(L_MAP + tokl * 4);
        char* outp = p.out + xo;
#pragma unroll
        for (int n = 0; n < 3; ++n) {
            if (n == 2 && kh) break;
            f32x4 a = acc[n] + lds_ld<f32x4>(rb + n * 1024);
            u32x2 xv = n == 0 ? xr0 : n == 1 ? xr1 : xr2;
            f32x4 bb = {bflo(xv[0]), bfhi(xv[0]), bflo(xv[1]), bfhi(xv[1])};
            if (slot >= 0) bb += lds_ld<f32x4>(L_YB + slot * 320 + kh * 192 + n * 64 + kg * 16);
            f32x4 rr = g * a + og * bb;
            u32x2 ov = {pk2(rr[0], rr[1]), pk2(rr[2], rr[3])};
            *(u32x2*)(outp + n * 32) = ov;
        }
        STAMP(10);
    }
}

extern "C" void kernel_launch(void* const* d_in, const int* in_sizes, int n_in,
                              void* d_out, int out_size, void* d_ws, size_t ws_size,
                              hipStream_t stream) {
    Params p;
    p.x = (const char*)d_in[0]; p.wqkv = (const char*)d_in[1]; p.wo = (const char*)d_in[2];
    p.wr = (const char*)d_in[4]; p.wg = (const char*)d_in[5]; p.wu = (const char*)d_in[6];
    p.wd = (const char*)d_in[7]; p.gw = (const char*)d_in[8];
    p.out = (char*)d_out; p.ws = (char*)d_ws;
    (void)hipFuncSetAttribute((const void*)fused_kernel, hipFuncAttributeMaxDynamicSharedMemorySize, LDS_BYTES);
    fused_kernel<<<256, 512, LDS_BYTES, stream>>>(p);
}
```

```cpp
#include <hip/hip_runtime.h>
#include <hip/hip_bf16.h>

typedef unsigned u32;
typedef unsigned long long u64;
typedef __attribute__((ext_vector_type(2))) unsigned u32x2;
typedef __attribute__((ext_vector_type(4))) unsigned u32x4;
typedef __attribute__((ext_vector_type(2))) float f32x2;
typedef __attribute__((ext_vector_type(4))) float f32x4;
typedef __attribute__((ext_vector_type(16))) float f32x16;
typedef __attribute__((ext_vector_type(2))) __bf16 bf16x2;
typedef __attribute__((ext_vector_type(8))) __bf16 bf16x8;
typedef __attribute__((ext_vector_type(4))) short s16x4;
#define LAS __attribute__((address_space(3)))
#define DI __device__ __forceinline__

constexpr u32 WS_EPOCH = 0;
constexpr u32 WS_RG    = 4096;
constexpr u32 WS_SEL   = 20480;
constexpr u32 WS_FLY   = 24576;
constexpr u32 WS_FL1   = 32768;
constexpr u32 WS_FL2   = 65536;
constexpr u32 WS_YP    = 262144;
constexpr u32 WS_Q     = 1u << 20;
constexpr u32 SZ_T     = 2621440;
constexpr u32 WS_K     = WS_Q + SZ_T;
constexpr u32 WS_V     = WS_K + SZ_T;
constexpr u32 WS_O     = WS_V + SZ_T;
constexpr u32 L_XS   = 131072;
constexpr u32 L_H    = L_XS + 10496;
constexpr u32 L_CAND = L_H + 2304;
constexpr u32 L_CNT  = L_CAND + 8192 + 128;
constexpr u32 L_MISC = L_CNT + 16;
constexpr u32 L_K0   = 102400;
constexpr u32 L_K1   = L_K0 + 16384;
constexpr u32 L_V    = L_K1 + 4096;
constexpr u32 L_RED  = L_K0;
constexpr u32 L_YB   = 145408;
constexpr u32 L_MAP  = L_YB + 2560;
constexpr u32 L_YT   = L_MAP + 256;
constexpr u32 L_TAG  = L_YT + 8192;
constexpr u32 LDS_BYTES = 160 * 1024;
constexpr int SPIN_MAX = 1 << 17;

struct Params {
    const char *x, *wqkv, *wo, *wr, *wg, *wu, *wd, *gw;
    char *out, *ws;
};

template <class T> DI T lds_ld(u32 a) { return *(const LAS T*)(a); }
template <class T> DI void lds_st(u32 a, T v) { *(LAS T*)(a) = v; }
DI s16x4 lds_tr(u32 a) { return __builtin_amdgcn_ds_read_tr16_b64_v4i16((LAS s16x4*)a); }
DI bf16x8 cat8(s16x4 a, s16x4 b) {
    u32x2 x = __builtin_bit_cast(u32x2, a), y = __builtin_bit_cast(u32x2, b);
    u32x4 r = {x[0], x[1], y[0], y[1]};
    return __builtin_bit_cast(bf16x8, r);
}
DI u32 pk2(float a, float b) {
    f32x2 v = {a, b};
    return __builtin_bit_cast(u32, __builtin_convertvector(v, bf16x2));
}
DI float u2f(u32 v) { return __builtin_bit_cast(float, v); }
DI u32 f2u(float v) { return __builtin_bit_cast(u32, v); }
DI float bflo(u32 v) { return __builtin_bit_cast(float, v << 16); }
DI float bfhi(u32 v) { return __builtin_bit_cast(float, v & 0xffff0000u); }
DI float rbf(float a) { return bflo(pk2(a, a)); }
DI float sigm(float a) { return __builtin_amdgcn_rcpf(1.0f + __builtin_amdgcn_exp2f(-1.44269504f * a)); }
DI u32 uni(u32 v) { return __builtin_amdgcn_readfirstlane(v); }

DI void dma16(u32 voff, const char* base, u32 dst) {
    asm volatile("s_mov_b32 m0, %2\n\ts_nop 0\n\tglobal_load_lds_dwordx4 %0, %1"
                 :: "v"(voff), "s"(base), "s"(dst) : "memory");
}
template <int OFF> DI u32x4 ld4(u32 voff, const char* base) {
    u32x4 d; asm volatile("global_load_dwordx4 %0, %1, %2 offset:%3" : "=v"(d) : "v"(voff), "s"(base), "i"(OFF) : "memory"); return d; }
template <int OFF> DI u32 ld1(u32 voff, const char* base) {
    u32 d; asm volatile("global_load_dword %0, %1, %2 offset:%3" : "=v"(d) : "v"(voff), "s"(base), "i"(OFF) : "memory"); return d; }
template <int OFF> DI u32x2 ld2(u32 voff, const char* base) {
    u32x2 d; asm volatile("global_load_dwordx2 %0, %1, %2 offset:%3" : "=v"(d) : "v"(voff), "s"(base), "i"(OFF) : "memory"); return d; }
template <int OFF> DI u32x4 ld4c(u32 voff, const char* base) {
    u32x4 d; asm volatile("global_load_dwordx4 %0, %1, %2 offset:%3 sc1" : "=v"(d) : "v"(voff), "s"(base), "i"(OFF) : "memory"); return d; }
template <int OFF> DI u32x2 ld2c(u32 voff, const char* base) {
    u32x2 d; asm volatile("global_load_dwordx2 %0, %1, %2 offset:%3 sc1" : "=v"(d) : "v"(voff), "s"(base), "i"(OFF) : "memory"); return d; }
template <int OFF> DI u32 ld1c(u32 voff, const char* base) {
    u32 d; asm volatile("global_load_dword %0, %1, %2 offset:%3 sc1" : "=v"(d) : "v"(voff), "s"(base), "i"(OFF) : "memory"); return d; }
template <int OFF> DI void st4c(u32 voff, u32x4 v, char* base) {
    asm volatile("global_store_dwordx4 %0, %1, %2 offset:%3 sc1\n\ts_nop 1" :: "v"(voff), "v"(v), "s"(base), "i"(OFF) : "memory"); }
template <int OFF> DI void st2c(u32 voff, u32x2 v, char* base) {
    asm volatile("global_store_dwordx2 %0, %1, %2 offset:%3 sc1" :: "v"(voff), "v"(v), "s"(base), "i"(OFF) : "memory"); }
template <int OFF> DI void st1c(u32 voff, u32 v, char* base) {
    asm volatile("global_store_dword %0, %1, %2 offset:%3 sc1" :: "v"(voff), "v"(v), "s"(base), "i"(OFF) : "memory"); }
DI void st_flag(u32 v, char* addr) {
    asm volatile("s_mov_b64 exec, 1\n\tglobal_store_dword %0, %1, %2 sc1\n\ts_mov_b64 exec, -1" :: "v"(0u), "v"(v), "s"(addr) : "memory"); }
template <int OFF> DI void st4p(u32 voff, u32x4 v, char* base) {
    asm volatile("global_store_dwordx4 %0, %1, %2 offset:%3\n\ts_nop 1" :: "v"(voff), "v"(v), "s"(base), "i"(OFF) : "memory"); }
#define WAITVM(n) asm volatile("s_waitcnt vmcnt(" #n ")" ::: "memory")
#define BAR() asm volatile("s_waitcnt lgkmcnt(0)\n\ts_barrier" ::: "memory")
#define PIN(x) asm volatile("" : "+v"(x))

#define ROT ((tl & 1) * 5 + (jl & 1) * 2 + (tl >> 1) * 2)
#define PSLEEP 2
DI void poll_flags(u32 voff, const char* base, u32 tag, bool need) {
#pragma unroll 1
    for (int it = 0; it < SPIN_MAX; ++it) {
        u32 v;
        asm volatile("global_load_dword %0, %1, %2 sc1\n\ts_waitcnt vmcnt(0)" : "=v"(v) : "v"(voff), "s"(base) : "memory");
        if (__builtin_amdgcn_ballot_w64(need && v != tag) == 0) break;
        __builtin_amdgcn_s_sleep(PSLEEP);
    }
}
#define PSTG "5"
DI void poll_pipe(u32 voff, const char* base, u32 tag, u32& a, u32& b, u32& c) {
    u32 it = SPIN_MAX;
    asm volatile("global_load_dword %0, %4, %5 sc1\n\ts_sleep " PSTG "\n\tglobal_load_dword %1, %4, %5 sc1\n\ts_sleep " PSTG "\n"
                 "1:\n\tglobal_load_dword %2, %4, %5 sc1\n\ts_waitcnt vmcnt(2)\n\tv_cmp_ne_u32 vcc, %6, %0\n\ts_cbranch_vccz 2f\n\t"
                 "global_load_dword %0, %4, %5 sc1\n\ts_waitcnt vmcnt(2)\n\tv_cmp_ne_u32 vcc, %6, %1\n\ts_cbranch_vccz 2f\n\t"
                 "global_load_dword %1, %4, %5 sc1\n\ts_waitcnt vmcnt(2)\n\tv_cmp_ne_u32 vcc, %6, %2\n\ts_cbranch_vccz 2f\n\t"
                 "s_sub_u32 %3, %3, 1\n\ts_cbranch_scc0 1b\n2:"
                 : "=&v"(a), "=&v"(b), "=&v"(c), "+s"(it) : "v"(voff), "s"(base), "s"(tag) : "memory", "vcc", "scc");
}
DI u32 poll_granule(u32 voff, const char* base, u32 tag) {
    u32x2 v;
#pragma unroll 1
    for (int it = 0; it < SPIN_MAX; ++it) {
        asm volatile("global_load_dwordx2 %0, %1, %2 sc1\n\ts_waitcnt vmcnt(0)" : "=v"(v) : "v"(voff), "s"(base) : "memory");
        if (__builtin_amdgcn_ballot_w64(v[1] != tag) == 0) break;
        __builtin_amdgcn_s_sleep(2);
    }
    return v[0];
}
template <int CTRL> DI float dpp_add(float v) {
    return v + __builtin_bit_cast(float, __builtin_amdgcn_update_dpp(0, __builtin_bit_cast(int, v), CTRL, 0xf, 0xf, true));
}
template <int CTRL> DI u32 dpp_max(u32 v) {
    u32 o = (u32)__builtin_amdgcn_update_dpp(0, (int)v, CTRL, 0xf, 0xf, true);
    return v > o ? v : o;
}
DI void count_gt(u32& n, u32x4 c, u32 k) {
    u64 a, b, d, e;
    asm("v_cmp_gt_u32 %1, %5, %9\n\tv_cmp_gt_u32 %2, %6, %9\n\tv_cmp_gt_u32 %3, %7, %9\n\tv_cmp_gt_u32 %4, %8, %9\n\t"
        "v_addc_co_u32 %0, vcc, 0, %0, %1\n\tv_addc_co_u32 %0, vcc, 0, %0, %2\n\tv_addc_co_u32 %0, vcc, 0, %0, %3\n\ts_nop 0\n\tv_addc_co_u32 %0, vcc, 0, %0, %4"
        : "+v"(n), "=&s"(a), "=&s"(b), "=&s"(d), "=&s"(e) : "v"(c[0]), "v"(c[1]), "v"(c[2]), "v"(c[3]), "v"(k) : "vcc");
}

struct Frags { bf16x8 x[4], a[4], b[4]; };
DI void frag_read(Frags& f, u32 x0, u32 w0) {
#pragma unroll
    for (int k = 0; k < 4; ++k) {
        f.x[k] = lds_ld<bf16x8>(x0 ^ (k * 32));
        f.a[k] = cat8(lds_tr(w0 + k * 4096), lds_tr(w0 + k * 4096 + 1024));
        f.b[k] = cat8(lds_tr((w0 ^ 64) + k * 4096), lds_tr((w0 ^ 64) + k * 4096 + 1024));
    }
}
#define SB() __builtin_amdgcn_sched_barrier(0)
template <bool SWAP> DI f32x16 mma(bf16x8 wf, bf16x8 xf, const f32x16& c) {
    return SWAP ? __builtin_amdgcn_mfma_f32_32x32x16_bf16(xf, wf, c, 0, 0, 0) : __builtin_amdgcn_mfma_f32_32x32x16_bf16(wf, xf, c, 0, 0, 0);
}
template <bool SWAP>
DI void gemm_stage(Frags& n, const Frags& o, u32 x0, u32 w0, f32x16& c0, f32x16& c1) {
#define RX(k) n.x[k] = lds_ld<bf16x8>(x0 ^ ((k) * 32));
#define RA(k) n.a[k] = cat8(lds_tr(w0 + (k) * 4096), lds_tr(w0 + (k) * 4096 + 1024));
#define RB(k) n.b[k] = cat8(lds_tr(w1 + (k) * 4096), lds_tr(w1 + (k) * 4096 + 1024));
#define M0(k) SB(); c0 = mma<SWAP>(o.a[k], o.x[k], c0); SB();
#define M1(k) SB(); c1 = mma<SWAP>(o.b[k], o.x[k], c1); SB();
    const u32 w1 = w0 ^ 64;
    M0(0) RX(0) RA(0)
    M1(0) RB(0) RX(1)
    M0(1) RA(1) RB(1)
    M1(1) RX(2) RA(2)
    M0(2) RB(2) RX(3)
    M1(2) RA(3) RB(3)
    M0(3)
    M1(3)
}
template <bool SWAP>
DI void gemm_loop(u32 w, u32 l, f32x16& c0, f32x16& c1) {
    u32 tw = w >> 1, cw = w & 1, h = l >> 5, row = tw * 32 + (l & 31);
    u32 xa = row * 128 + ((h ^ ((row >> 1) & 7)) << 4);
    u32 q = (l >> 2) & 3, pp = l & 3, g1 = (l >> 4) & 1;
    u32 ch = SWAP ? (pp >> 1) : (pp & 1), hf = SWAP ? (pp & 1) : (pp >> 1);
    u32 wa = 16384 + (8 * h + q) * 256 + (((8 * cw + 2 * g1 + ch) ^ (q << 2)) << 4) + hf * 8;
    Frags fa, fb;
    BAR();
    frag_read(fa, xa, wa);
#pragma unroll 1
    for (int s = 1; s < 9; s += 2) {
        u32 o1 = (s & 3) << 15, o2 = ((s + 1) & 3) << 15;
        BAR();
        gemm_stage<SWAP>(fb, fa, xa + o1, wa + o1, c0, c1);
        BAR();
        gemm_stage<SWAP>(fa, fb, xa + o2, wa + o2, c0, c1);
    }
    BAR();
    gemm_stage<SWAP>(fb, fa, xa + 32768, wa + 32768, c0, c1);
#pragma unroll
    for (int k = 0; k < 4; ++k) c0 = mma<SWAP>(fb.a[k], fb.x[k], c0);
#pragma unroll
    for (int k = 0; k < 4; ++k) c1 = mma<SWAP>(fb.b[k], fb.x[k], c1);
}
DI u32x4 pk8(const f32x16& c, int o) {
    u32x4 r = {pk2(c[o], c[o + 1]), pk2(c[o + 2], c[o + 3]), pk2(c[o + 4], c[o + 5]), pk2(c[o + 6], c[o + 7])};
    return r;
}
constexpr u32 EMPTY = 0xffffffffu;
DI u32 pk2h(float a, float b) { return min(pk2(a, b), EMPTY - 1); }
DI u32x4 pk8h(const f32x16& c, int o) {
    u32x4 r = {pk2h(c[o], c[o + 1]), pk2h(c[o + 2], c[o + 3]), pk2h(c[o + 4], c[o + 5]), pk2h(c[o + 6], c[o + 7])};
    return r;
}
DI u32 mx4(u32 m, const u32x4& v) { return max(max(max(max(m, v[0]), v[1]), v[2]), v[3]); }
DI u32 mx2(u32 m, const u32x2& v) { return max(max(m, v[0]), v[1]); }
DI f32x16 qk_tile(u32 ka, u32 kb, bf16x8 q0, s16x4 q1, const f32x16& c) {
    f32x16 s = __builtin_amdgcn_mfma_f32_32x32x16_bf16(lds_ld<bf16x8>(ka), q0, c, 0, 0, 0);
    return __builtin_amdgcn_mfma_f32_32x32x8bf16_1k(lds_ld<s16x4>(kb), q1, s, 0, 0, 0);
}
DI f32x16 pv_tile(u32 va, f32x16 s, const f32x16& o) {
#pragma unroll
    for (int i = 0; i < 16; ++i) s[i] = __builtin_amdgcn_exp2f(s[i]);
    f32x16 r = __builtin_amdgcn_mfma_f32_32x32x16_bf16(lds_ld<bf16x8>(va), __builtin_bit_cast(bf16x8, pk8(s, 0)), o, 0, 0, 0);
    return __builtin_amdgcn_mfma_f32_32x32x16_bf16(lds_ld<bf16x8>(va + 672), __builtin_bit_cast(bf16x8, pk8(s, 8)), r, 0, 0, 0);
}

__global__ void __launch_bounds__(768, 3) fused_kernel(Params p) {
    const u32 b = blockIdx.x, tid = threadIdx.x, l = tid & 63, w = uni(tid >> 6);
    const u32 xcd = b & 7, rk = b >> 3;
    const bool isg = rk < 30;
    const u32 e = xcd & 1, lo = rk < 16, q7 = rk - 16;
    const u32 tl = lo ? (rk >> 3) + 2 * e : (q7 >= 7) + 2 - 2 * e, jl = lo ? rk & 7 : q7 >= 7 ? q7 - 7 : q7;
    const u32 t1 = (xcd >> 1) * 4 + tl, j1 = jl + (lo ? 7 : 8) * e;
    const u32 fi = xcd * 2 + (rk - 30);
    const u32 tt = xcd * 4 + (rk >> 3), cgp = rk & 7;

    if (__builtin_expect(w >= 8, 1)) {
        const u32 lw = w - 8;
        if (isg || fi < 12) {
            u32 va[4], vb[4], sa, sb;
            const char *ra, *rb, *pfa = p.x;
            int ks = 0;
            const u32 wr_ = lw * 16 + (l >> 4), wc = ((l & 15) ^ ((l >> 4) << 2)) << 4;
            if (isg) {
                u32 row = lw * 32 + (l >> 3), f = (row >> 1) & 7;
#pragma unroll
                for (int i = 0; i < 4; ++i) {
                    va[i] = (row + 8 * i) * 1280 + (((l & 7) ^ f ^ ((i & 1) * 4)) << 4);
                    vb[i] = (wr_ + 4 * i) * 3840 + wc;
                }
                ks = ROT;
                if (ks >= 10) ks -= 10;
                ra = p.x + (size_t)t1 * 128 * 1280 + ks * 128;
                rb = p.wqkv + j1 * 256 + ks * (64 * 3840);
                sa = 128; sb = 64 * 3840;
                u32 n = min((rk * 4 + lw) * 48 + l, 5631u), hi = n >= 2816, m = n - hi * 2816, fs = (xcd < 6 ? xcd : 0) * 2 + hi;
                pfa = m < 1536 ? ((m & 1) ? p.wu : p.wg) + ((256 + (m >> 2)) * 3072 + fs * 256 + (m & 2) * 64)
                               : p.wd + (fs * 163840 + (m - 1536) * 128);
            } else {
#pragma unroll
                for (int i = 0; i < 4; ++i) va[i] = vb[i] = (wr_ + 4 * i) * 3072 + (wc ^ ((i >> 1) << 5));
                ra = p.wg + fi * 256; rb = p.wu + fi * 256;
                sa = sb = 64 * 3072;
            }
            const int nst = isg ? 10 : 15;
            u32 pf = 0;
            const int lag = isg ? 2 : 3;
#pragma unroll 1
            for (int s = isg ? -2 : -4; s < nst; ++s) {
                int i = s + lag + (!isg && s < 0);
                if (s >= 0) {
                    if (s == nst - 1) { if (isg) WAITVM(1); else WAITVM(0); }
                    else if (isg || s == nst - 2) WAITVM(8);
                    else if (s == 0) { BAR(); BAR(); WAITVM(24); i = nst; }
                    else WAITVM(16);
                    BAR();
                }
                if (i < nst) {
                    if (i == 10) {
#pragma unroll
                        for (int k = 0; k < 4; ++k) va[k] = vb[k] = (wr_ + 4 * k) * 1280 + (wc ^ ((k >> 1) << 5));
                        ra = p.wd + (size_t)fi * 128 * 1280; rb = ra + 64 * 1280;
                        sa = sb = 256;
                    }
                    u32 d = (i & 3) * 32768 + lw * 4096;
#pragma unroll
                    for (int k = 0; k < 4; ++k) dma16(va[k], ra, d + k * 1024);
#pragma unroll
                    for (int k = 0; k < 4; ++k) dma16(vb[k], rb, d + 16384 + k * 1024);
                    if (++ks == 10 && isg) { ks = 0; ra -= 9 * sa; rb -= 9 * sb; } else { ra += sa; rb += sb; }
                    if (i == 9 && isg)
                        asm volatile("s_mov_b32 exec_hi, 0xffff\n\tglobal_load_dword %0, %1, off\n\ts_mov_b32 exec_hi, -1" : "+v"(pf) : "v"(pfa) : "memory");
                }
            }
            asm volatile("s_waitcnt vmcnt(0)" : "+v"(pf) :: "memory");
            BAR();
            if (isg) {
                u32 pa = 0, pb = 0, pc = 0;
                if (lw == 0) {
                    const bool pair = rk >= 28;
                    const u32 u = pair ? 0 : 1 + (rk >> 2), head = xcd * 4 + (pair ? (rk & 1) * 2 : rk & 3);
                    u32 id = l >> 3, ti = (l >> 1) & 3, hx = head + (pair ? ti >> 1 : 0);
                    u32 cc = (head >> 1) * 5 + (hx & 1) * 2 + (l & 1) * 2, tq = (pair || id == 0) ? 2 * u + (ti & 1) : 2 * u - 2 + ti;
                    u32 tag = uni(lds_ld<u32>(L_TAG));
                    if (l < 24) poll_pipe((tq * 16 + id * 5 + (cc >> 4)) * 128, p.ws + WS_FL1, tag, pa, pb, pc);
                }
                BAR();
                asm volatile("s_waitcnt vmcnt(0)" : "+v"(pa), "+v"(pb), "+v"(pc) :: "memory");
                BAR();
            }
        }
#pragma unroll 1
        for (u32 i = lw * 25; i < lw * 25 + 25; ++i) {
            u32 c = i * 64 + l;
            dma16(((c * 6554) >> 16) * 1120 + c * 16, p.wo + cgp * 160, i * 1024);
        }
        WAITVM(0);
        BAR();
        {
            u32 pa = 0, pb = 0, pc = 0, tag = uni(lds_ld<u32>(L_TAG));
            if (l < 16) poll_pipe(((tt * 2 + (lw & 1)) * 32 + (lw >> 1) * 16 + l) * 64, p.ws + WS_FL2, tag, pa, pb, pc);
            if (l == 0) lds_st<u32>(L_TAG + 4 + lw * 4, 1u);
            asm volatile("s_waitcnt vmcnt(0) lgkmcnt(0)" : "+v"(pa), "+v"(pb), "+v"(pc) :: "memory");
        }
        return;
    }

    u32 cpf = 0;
    {
        u64 pc; asm volatile("s_getpc_b64 %0" : "=s"(pc));
        if (w == 7) cpf = ld1<0>(((rk < 20 ? rk : 0) * 8 + (l & 7)) * 128, (const char*)(pc & ~127ull));
    }
    u32 ep = ld1c<0>(0u, p.ws);
    const char* xrow = p.x + (size_t)(b * 8 + w) * 1280;
    u32 l20 = l * 20;
    u32x4 rx4 = ld4<0>(l20, xrow), rw4 = ld4<0>(l20, p.wr), rg4 = ld4<0>(l20, p.gw);
    u32 rx1 = ld1<16>(l20, xrow), rw1 = ld1<16>(l20, p.wr), rg1 = ld1<16>(l20, p.gw);
    asm volatile("s_waitcnt vmcnt(0)" : "+v"(ep), "+v"(rx4), "+v"(rw4), "+v"(rg4), "+v"(rx1), "+v"(rw1), "+v"(rg1), "+v"(cpf) :: "memory");
    const u32 tag = uni(ep) + 1;
    if (tid < 5) lds_st<u32>(L_TAG + tid * 4, tid ? 0u : tag);
    {
        float ar = 0.f, ag = 0.f;
#pragma unroll
        for (int i = 0; i < 5; ++i) {
            bf16x2 xv = __builtin_bit_cast(bf16x2, i < 4 ? rx4[i & 3] : rx1);
            ar = __builtin_amdgcn_fdot2_f32_bf16(xv, __builtin_bit_cast(bf16x2, i < 4 ? rw4[i & 3] : rw1), ar, false);
            ag = __builtin_amdgcn_fdot2_f32_bf16(xv, __builtin_bit_cast(bf16x2, i < 4 ? rg4[i & 3] : rg1), ag, false);
        }
        auto sw = __builtin_amdgcn_permlane32_swap(f2u(ar), f2u(ag), false, false);
        float v = u2f(sw[0]) + u2f(sw[1]);
        v = dpp_add<0x111>(v); v = dpp_add<0x112>(v); v = dpp_add<0x114>(v); v = dpp_add<0x118>(v);
        v = dpp_add<0x142>(v);
        float rs = __builtin_bit_cast(float, __builtin_amdgcn_readlane(__builtin_bit_cast(int, v), 31));
        float gs = __builtin_bit_cast(float, __builtin_amdgcn_readlane(__builtin_bit_cast(int, v), 63));
        u32x2 gr = {pk2(sigm(rbf(gs)), rs), tag};
        if (l == 0) st2c<0>((b * 8 + w) * 8, gr, p.ws + WS_RG);
    }

    if (isg) {
        f32x16 c0 = {}, c1 = {};
        const u32 kind = j1 / 5, jc = j1 - kind * 5, tile = t1 * 4 + (w >> 1);
        if (kind == 2) {
            gemm_loop<true>(w, l, c0, c1);
            u32 col = jc * 128 + (w & 1) * 64 + (l & 31), h = l >> 5;
            u32 hd0 = (col * 3277) >> 16, hd1 = ((col + 32) * 3277) >> 16;
            u32 v0 = hd0 * (64 * 1280) + h * 320 + (col - hd0 * 20) * 16;
            u32 v1 = hd1 * (64 * 1280) + h * 320 + (col + 32 - hd1 * 20) * 16;
            char* base = p.ws + WS_V + tile * 1280;
            st4c<0>(v0, pk8(c0, 0), base); st4c<640>(v0, pk8(c0, 8), base);
            st4c<0>(v1, pk8(c1, 0), base); st4c<640>(v1, pk8(c1, 8), base);
        } else {
            gemm_loop<false>(w, l, c0, c1);
            if (kind == 0) { c0 *= 0.32259784f; c1 *= 0.32259784f; }
            char* base = p.ws + WS_Q + kind * SZ_T + (tile * 80 + jc * 16 + (w & 1) * 8) * 512;
            u32 vo = l * 16;
            st4c<0>(vo, pk8(c0, 0), base); st4c<1024>(vo, pk8(c0, 8), base);
            st4c<2048>(vo, pk8(c1, 0), base); st4c<3072>(vo, pk8(c1, 8), base);
        }
        WAITVM(0);
        BAR();
        if (w == 0) st_flag(tag, p.ws + WS_FL1 + (t1 * 16 + j1) * 128);
    } else if (fi < 12) {
        u32 key[4];
        {
            u32x4 g0, g1, zz = {};
            lds_st<u32x4>(L_CAND + tid * 16, zz);
            if (tid < 9) lds_st<u32x4>(L_CAND + 8192 + tid * 16, zz);
            const char* gb = p.ws + WS_RG;
#pragma unroll 1
            for (int it = 0; it < SPIN_MAX; ++it) {
                g0 = ld4c<0>(tid * 16, gb); g1 = ld4c<0>(tid * 16, gb + 8192);
                asm volatile("s_waitcnt vmcnt(0)" : "+v"(g0), "+v"(g1) :: "memory");
                if (__builtin_amdgcn_ballot_w64(g0[1] != tag || g0[3] != tag || g1[1] != tag || g1[3] != tag) == 0) break;
                __builtin_amdgcn_s_sleep(1);
            }
#pragma unroll
            for (int i = 0; i < 4; ++i) {
                u32 v = (i == 0 ? g0[0] : i == 1 ? g0[2] : i == 2 ? g1[0] : g1[2]) >> 16;
                v ^= (v & 0x8000u) ? 0xffffu : 0x8000u;
                key[i] = (v << 16) | (2047 - ((i >> 1) * 1024 + tid * 2 + (i & 1)));
            }
        }
        u32 m = max(max(key[0], key[1]), max(key[2], key[3]));
        m = dpp_max<0x111>(m); m = dpp_max<0x112>(m); m = dpp_max<0x114>(m); m = dpp_max<0x118>(m);
        m = dpp_max<0x142>(m); m = dpp_max<0x143>(m);
        if (l == 63) lds_st<u32>(L_MISC + w * 4, m);
        BAR();
        if (fi == 0 && tid == 0) st1c<0>(0u, tag, p.ws + WS_EPOCH);
        {
            u32x4 ma = lds_ld<u32x4>(L_MISC), mb = lds_ld<u32x4>(L_MISC + 16);
            u32 thr = min(min(min(ma[0], ma[1]), min(ma[2], ma[3])), min(min(mb[0], mb[1]), min(mb[2], mb[3])));
            u32 pos[4], cnt = 0;
#pragma unroll
            for (int i = 0; i < 4; ++i) {
                u64 bm = __builtin_amdgcn_ballot_w64(key[i] >= thr);
                pos[i] = cnt + __builtin_amdgcn_mbcnt_hi((u32)(bm >> 32), __builtin_amdgcn_mbcnt_lo((u32)bm, 0));
                cnt += __builtin_popcountll(bm);
            }
            u32 base;
            asm volatile("s_mov_b64 exec, 1\n\tds_add_rtn_u32 %0, %1, %2\n\ts_mov_b64 exec, -1\n\ts_waitcnt lgkmcnt(0)"
                         : "=&v"(base) : "v"(L_CNT), "v"(cnt) : "memory");
            base = uni(base);
#pragma unroll
            for (int i = 0; i < 4; ++i) if (key[i] >= thr) lds_st<u32>(L_CAND + (base + pos[i]) * 4, key[i]);
        }
        BAR();
        {
            const u32 nv = lds_ld<u32>(L_CNT);
            u32 k = 0, i0 = 0;
#pragma unroll 1
            do {
                u32 kc = lds_ld<u32>(L_CAND + (i0 + (l & 31)) * 4), rank = 0, j0 = 0;
#pragma unroll 1
                do {
                    u32 ja = L_CAND + j0 * 4 + (l >> 5) * 64;
                    u32x4 c0 = lds_ld<u32x4>(ja), c1 = lds_ld<u32x4>(ja + 16), c2 = lds_ld<u32x4>(ja + 32), c3 = lds_ld<u32x4>(ja + 48);
                    count_gt(rank, c0, kc); count_gt(rank, c1, kc); count_gt(rank, c2, kc); count_gt(rank, c3, kc);
                    j0 += 32;
                } while (j0 < uni(nv));
                auto rs = __builtin_amdgcn_permlane32_swap(rank, rank, false, false);
                u64 bm = __builtin_amdgcn_ballot_w64(rs[0] + rs[1] == w && i0 + (l & 31) < nv);
                if (bm) k = __builtin_amdgcn_readlane(kc, __builtin_ctzll(bm));
                i0 += 32;
            } while (i0 < uni(nv));
            if (fi == 0 && l == 0) { u32x2 g = {k, tag}; st2c<0>(w * 8, g, p.ws + WS_SEL); }
            const char* xs = p.x + (size_t)(2047 - (k & 0xffffu)) * 1280;
            dma16(l * 16, xs, L_XS + w * 1312);
            if (l < 16) dma16(l * 16, xs + 1024, L_XS + w * 1312 + 1024);
        }
        WAITVM(0);
        const u32 kg = l >> 4, q = (l >> 2) & 3, pp = l & 3;
        u32 wa = (8 * kg + q) * 256 + (((2 * w + (pp >> 1)) ^ (q << 2) ^ ((kg & 1) << 1)) << 4) + (pp & 1) * 8;
        u32 xa = L_XS + (l & 7) * 1312 + kg * 16;
        f32x4 cg = {}, cu = {};
#define FFN_READ(s_)                                                                                        \
            BAR();                                                                                          \
            u32 a = wa + (((s_) & 3) << 15);                                                                \
            bf16x8 wf[4];                                                                                   \
            _Pragma("unroll") for (int k = 0; k < 4; ++k) wf[k] = cat8(lds_tr(a + k * 8192), lds_tr(a + k * 8192 + 1024)); \
            bf16x8 x0 = lds_ld<bf16x8>(xa), x1 = lds_ld<bf16x8>(xa + 64);
#pragma unroll 1
        for (int s = 0; s < 10; ++s) {
            FFN_READ(s)
            cg = __builtin_amdgcn_mfma_f32_16x16x32_bf16(wf[0], x0, cg, 0, 0, 0);
            cu = __builtin_amdgcn_mfma_f32_16x16x32_bf16(wf[2], x0, cu, 0, 0, 0);
            cg = __builtin_amdgcn_mfma_f32_16x16x32_bf16(wf[1], x1, cg, 0, 0, 0);
            cu = __builtin_amdgcn_mfma_f32_16x16x32_bf16(wf[3], x1, cu, 0, 0, 0);
            xa += 128;
        }
        {
            float hv[4];
#pragma unroll
            for (int i = 0; i < 4; ++i) { float g = rbf(cg[i]); hv[i] = rbf(g * sigm(g)) * rbf(cu[i]); }
            u32x2 hp = {pk2(hv[0], hv[1]), pk2(hv[2], hv[3])};
            if ((l & 15) < 8) lds_st<u32x2>(L_H + (l & 7) * 288 + w * 32 + kg * 8, hp);
            xa = L_H + (l & 7) * 288 + kg * 16;
        }
        const u32 yo = (l & 7) * 2560 + w * 64 + kg * 16;
        char* yb = p.ws + WS_YP + fi * 20480;
#pragma unroll 1
        for (int s = 10; s < 15; ++s) {
            FFN_READ(s)
            bf16x8 x2 = lds_ld<bf16x8>(xa + 128), x3 = lds_ld<bf16x8>(xa + 192);
            f32x4 y = {};
            y = __builtin_amdgcn_mfma_f32_16x16x32_bf16(wf[0], x0, y, 0, 0, 0);
            y = __builtin_amdgcn_mfma_f32_16x16x32_bf16(wf[1], x1, y, 0, 0, 0);
            y = __builtin_amdgcn_mfma_f32_16x16x32_bf16(wf[2], x2, y, 0, 0, 0);
            y = __builtin_amdgcn_mfma_f32_16x16x32_bf16(wf[3], x3, y, 0, 0, 0);
            u32x4 yv = __builtin_bit_cast(u32x4, y);
            asm volatile("s_nop 8" : "+v"(yv));
            if ((l & 15) < 8) st4c<0>(yo, yv, yb);
            yb += 512;
        }
        WAITVM(0);
        BAR();
        if (tid == 0) st1c<0>(fi * 128, tag, p.ws + WS_FLY);
    }

    const u32 ts = w & 3, kh = w >> 2, kg = l >> 4, t16 = l & 15;
    const u32 tokl = ts * 16 + t16, token = tt * 64 + tokl;
    const u32 xo = token * 1280 + cgp * 160 + kg * 8 + kh * 64;
    u32x2 gv2, sk2, xr0, xr1, xr2;
    auto epi_issue = [&]() {
        gv2 = ld2c<0>(token * 8, p.ws + WS_RG); sk2 = ld2c<0>(w * 8, p.ws + WS_SEL);
        xr0 = ld2<0>(xo, p.x); xr1 = ld2<32>(xo, p.x); xr2 = ld2<128>(xo - kh * 64, p.x);
    };
    if (isg) {
        const bool pair = rk >= 28;
        const u32 u = pair ? 0 : 1 + (rk >> 2), head = xcd * 4 + (pair ? (rk & 1) * 2 : rk & 3);
        const u32 hkv = head + (pair && w < 4), k0 = pair ? (2 * w) & 7 : u * 8 - 8 + 2 * w;
        const u32 c5 = (head >> 1) * 5, h = l >> 5, tok = l & 31;
        u32 oi = (u * 8 + w) * 32 + head;
        const u32 oi2 = pair ? (7 - w) * 32 + head + 1 : oi;
        const char* qb = p.ws + WS_Q + ((u * 8 + w) * 80 + c5) * 512;
        const char* qb2 = p.ws + WS_Q + ((7 - w) * 80 + c5) * 512;
        const char* kb = p.ws + WS_K + (k0 * 80 + c5) * 512;
        const char* vb = p.ws + WS_V + (hkv * 64 + k0) * 1280;
        const u32 vm = l * 16, vs = 1024 + tok * 16, oq = head & 1, ok = hkv & 1;
        u32x4 km0, km1, v0, v1, v2, qm, qm2 = {};
        u32x2 ksm, qs, qs2 = {};
        u32 a0 = vm + ok * 1536, a1 = a0 + 40960, a2 = vs + ok * 8 + h * 40960, a3 = tok * 16, a4 = vm + oq * 1536, a5 = vs + oq * 8;
        PIN(a0); PIN(a1); PIN(a2); PIN(a3); PIN(a4); PIN(a5);
        BAR();
        km0 = ld4c<0>(a0, kb); km1 = ld4c<0>(a1, kb); ksm = ld2c<0>(a2, kb);
        v0 = ld4c<0>(vm, vb); v1 = ld4c<1024>(vm, vb); v2 = ld4c<2048>(a3, vb);
        qm = ld4c<0>(a4, qb); qs = ld2c<0>(a5, qb);
        if (pair) { qm2 = ld4c<1536>(vm, qb2); qs2 = ld2c<8>(vs, qb2); }
        f32x16 cd, ce;
#pragma unroll
        for (int i = 0; i < 16; ++i) {
            bool le = (u32)((i & 3) + 8 * (i >> 2)) + 4 * h <= tok;
            cd[i] = le ? 0.f : -1e30f;
            ce[i] = le ? -1e30f : 0.f;
        }
        const u32 lv = L_V + w * 2688, p1 = l + 64, p2 = tok + 128;
        u32 s0 = L_K0 + w * 2048 + l * 16, s1 = L_K1 + w * 512 + l * 8, s2 = lv + (l + ((l * 3277) >> 16)) * 16;
        u32 s3 = lv + (p1 + ((p1 * 3277) >> 16)) * 16;
        u32 s4 = l < 32 ? lv + (p2 + ((p2 * 3277) >> 16)) * 16 : l < 40 ? lv + (l - 32) * 336 + 320 : L_TAG + 32 + tok * 16;
        PIN(cd); PIN(ce); PIN(s0); PIN(s1); PIN(s2); PIN(s3); PIN(s4);
        asm volatile("s_waitcnt vmcnt(0)" : "+v"(qm), "+v"(qs), "+v"(qm2), "+v"(qs2), "+v"(km0), "+v"(km1), "+v"(ksm), "+v"(v0), "+v"(v1), "+v"(v2) :: "memory");
        lds_st<u32x4>(s0, km0);
        lds_st<u32x4>(s0 + 1024, km1);
        lds_st<u32x2>(s1, ksm);
        lds_st<u32x4>(s2, v0);
        lds_st<u32x4>(s3, v1);
#pragma unroll
        for (int i = 0; i < 4; ++i) v2[i] = h ? 0x3f803f80u : v2[i];
        lds_st<u32x4>(s4, v2);
        if (h) { qs[0] = 0; qs[1] = 0; qs2[0] = 0; qs2[1] = 0; }
        epi_issue();
        {
            const u32x4 em = {EMPTY, EMPTY, EMPTY, EMPTY};
            char *ob = p.ws + WS_O + oi * 1280, *ob2 = p.ws + WS_O + oi2 * 1280;
            st4c<0>(vm, em, ob);
            if (pair) st4c<0>(vm, em, ob2);
            if (l < 16) { st4c<1024>(vm, em, ob); if (pair) st4c<1024>(vm, em, ob2); }
        }
        BAR();
        u32 wq = w;
        int j0 = pair ? 8 - (int)w : 0;
        const int jce = pair ? -1 : 2;
        f32x16 cc = {};
#pragma unroll 1
        for (int pass = 0;; ++pass) {
            const bf16x8 q0 = __builtin_bit_cast(bf16x8, qm);
            const s16x4 q1 = __builtin_bit_cast(s16x4, qs);
            const u32 ka = L_K0 + wq * 1024 + l * 16, kb1 = L_K1 + wq * 256 + tok * 8, va = L_V + wq * 1344 + h * 336 + tok * 16;
            f32x16 o = {};
            const f32x16 z = {};
            {
                f32x16 s = qk_tile(ka + 8 * 1024, kb1 + 8 * 256, q0, q1, cd);
                u32 kp = ka + 7 * 1024, k1p = kb1 + 7 * 256, vp = va + 8 * 1344;
                bf16x8 vf0 = lds_ld<bf16x8>(vp), vf1 = lds_ld<bf16x8>(vp + 672), kf0 = lds_ld<bf16x8>(kp);
                s16x4 kf1 = lds_ld<s16x4>(k1p);
#pragma unroll 1
                for (int j = 8;; --j) {
                    float e[16];
#pragma unroll
                    for (int i = 0; i < 8; ++i) e[i] = __builtin_amdgcn_exp2f(s[i]);
                    u32x4 p0 = {pk2(e[0], e[1]), pk2(e[2], e[3]), pk2(e[4], e[5]), pk2(e[6], e[7])};
                    SB();
                    o = __builtin_amdgcn_mfma_f32_32x32x16_bf16(vf0, __builtin_bit_cast(bf16x8, p0), o, 0, 0, 0);
                    SB();
#pragma unroll
                    for (int i = 8; i < 16; ++i) e[i] = __builtin_amdgcn_exp2f(s[i]);
                    SB();
                    s = __builtin_amdgcn_mfma_f32_32x32x16_bf16(kf0, q0, cc, 0, 0, 0);
                    SB();
                    u32x4 p1 = {pk2(e[8], e[9]), pk2(e[10], e[11]), pk2(e[12], e[13]), pk2(e[14], e[15])};
                    kp -= 1024; vp -= 1344; k1p -= 256;
                    vf0 = lds_ld<bf16x8>(vp); kf0 = lds_ld<bf16x8>(kp);
                    SB();
                    s = __builtin_amdgcn_mfma_f32_32x32x8bf16_1k(kf1, q1, s, 0, 0, 0);
                    o = __builtin_amdgcn_mfma_f32_32x32x16_bf16(vf1, __builtin_bit_cast(bf16x8, p1), o, 0, 0, 0);
                    SB();
                    vf1 = lds_ld<bf16x8>(vp + 672); kf1 = lds_ld<s16x4>(k1p);
                    if (j == jce) { cc = ce; asm volatile("" : "+v"(cc)); }
                    if (j == j0) break;
                }
            }
            asm volatile("s_waitcnt vmcnt(0)" : "+v"(gv2), "+v"(sk2), "+v"(xr0), "+v"(xr1), "+v"(xr2) :: "memory");
            st_flag(tag, p.ws + WS_FL2 + oi * 64);
            auto lsw = __builtin_amdgcn_permlane32_swap(f2u(o[8]), f2u(o[8]), false, false);
            float lsum = u2f(lsw[1]);
            if (__builtin_expect(__builtin_amdgcn_ballot_w64(!(lsum >= 0x1p-64f && lsum <= 0x1p64f)) != 0, 0)) {
                float m = -3e38f;
#pragma unroll 1
                for (int j = j0; j <= 8; ++j) {
                    f32x16 s = qk_tile(ka + j * 1024, kb1 + j * 256, q0, q1, j == 8 ? cd : j == 0 ? ce : z);
#pragma unroll
                    for (int i = 0; i < 16; ++i) m = fmaxf(m, s[i]);
                }
                auto msw = __builtin_amdgcn_permlane32_swap(f2u(m), f2u(m), false, false);
                m = fmaxf(u2f(msw[0]), u2f(msw[1]));
                o = z;
#pragma unroll 1
                for (int j = j0; j <= 8; ++j) {
                    f32x16 s = qk_tile(ka + j * 1024, kb1 + j * 256, q0, q1, j == 8 ? cd : j == 0 ? ce : z);
                    o = pv_tile(va + j * 1344, s - m, o);
                }
                lsw = __builtin_amdgcn_permlane32_swap(f2u(o[8]), f2u(o[8]), false, false);
                lsum = u2f(lsw[1]);
            }
            float inv = __builtin_amdgcn_rcpf(lsum);
            o *= inv;
            char* ob = p.ws + WS_O + oi * 1280;
            st4c<0>(l * 16, pk8h(o, 0), ob);
            if (l < 32) { u32x2 sg = {pk2h(o[8], o[9]), pk2h(o[10], o[11])}; st2c<1024>(l * 8, sg, ob); }
            if (!pair || pass) break;
            qm = qm2; qs = qs2; wq = ~w; j0 = w + 1; oi = oi2;
        }
    } else {
        epi_issue();
        asm volatile("s_waitcnt vmcnt(0)" : "+v"(gv2), "+v"(sk2), "+v"(xr0), "+v"(xr1), "+v"(xr2) :: "memory");
    }

    {
        if (tid < 64) lds_st<int>(L_MAP + tid * 4, -1);
        u32 gv = gv2[0], sk = sk2[0];
        if (__builtin_amdgcn_ballot_w64(gv2[1] != tag || sk2[1] != tag) != 0) {
            gv = poll_granule(token * 8, p.ws + WS_RG, tag);
            sk = poll_granule(w * 8, p.ws + WS_SEL, tag);
        }
        sk = uni(sk);
        BAR();
        const u32 st = 2047 - (sk & 0xffffu);
        if ((st >> 6) == tt) {
            poll_flags(l < 12 ? l * 128 : 0u, p.ws + WS_FLY, tag, l < 12);
            u32 p3 = (l * 13) >> 8, qd = l - p3 * 20;
            u32 vo = (p3 < 3 ? p3 : 0) * 20480 + w * 2560 + cgp * 320 + qd * 16;
            const char* yb = p.ws + WS_YP;
            u32x4 y0 = ld4c<0>(vo, yb), y1 = ld4c<0>(vo + 61440, yb), y2 = ld4c<0>(vo + 122880, yb), y3 = ld4c<0>(vo + 184320, yb);
            asm volatile("s_waitcnt vmcnt(0)" : "+v"(y0), "+v"(y1), "+v"(y2), "+v"(y3) :: "memory");
            f32x4 ys = (__builtin_bit_cast(f32x4, y0) + __builtin_bit_cast(f32x4, y1)) + (__builtin_bit_cast(f32x4, y2) + __builtin_bit_cast(f32x4, y3));
            lds_st<f32x4>(L_YT + w * 1024 + l * 16, ys);
            asm volatile("s_waitcnt lgkmcnt(0)" ::: "memory");
            if (l < 20) {
                ys += lds_ld<f32x4>(L_YT + w * 1024 + l * 16 + 320) + lds_ld<f32x4>(L_YT + w * 1024 + l * 16 + 640);
                u32 rb = sk >> 16;
                rb ^= (rb & 0x8000u) ? 0x8000u : 0xffffu;
                float rsg = rbf(sigm(bflo(rb)));
#pragma unroll
                for (int i = 0; i < 4; ++i) ys[i] = rbf(rbf(ys[i]) * rsg);
                lds_st<f32x4>(L_YB + w * 320 + l * 16, ys);
            }
            if (l == 0) lds_st<int>(L_MAP + (st & 63) * 4, (int)w);
        }
        const u32 tile = tt * 2 + (ts >> 1), t32 = (ts & 1) * 16 + t16;
        const u32 q = (l >> 2) & 3, pp = l & 3;
        const u32 am = (kh * 320 + (kg >> 1) * 20 + (kg & 1) * 4 + q) * 160 + pp * 8;
        const u32 as = (kh * 320 + kg * 20 + 16 + q) * 160 + pp * 8;
        const char* ob = p.ws + WS_O + (tile * 32 + kh * 16) * 1280;
        const u32 vm = (kg >> 1) * 1280 + (kg & 1) * 512 + t32 * 16, vs = kg * 1280 + 1024 + t32 * 8;
        f32x4 acc[5];
#define P3RD(s)                                                                                             \
        _Pragma("unroll") for (int n = 0; n < 5; ++n) {                                                     \
            const u32 a_ = ((s) < 8 ? am + (s) * 6400 : as + ((s) - 8) * 25600) + n * 32;                   \
            wf[(s) & 3][n] = cat8(lds_tr(a_), lds_tr(a_ + ((s) < 8 ? 1280 : 12800)));                       \
        }
#define P3MM(s, B)                                                                                          \
        _Pragma("unroll") for (int n = 0; n < 5; ++n)                                                       \
            acc[n] = __builtin_amdgcn_mfma_f32_16x16x32_bf16(wf[(s) & 3][n], (B), acc[n], 0, 0, 0);         \
        SB();
#define P3MAIN(m, cnt)                                                                                      \
        asm volatile("s_waitcnt vmcnt(" #cnt ")" : "+v"(om[m]) :: "memory");                                \
        mo = mx4(mo, om[m]);                                                                                \
        P3MM(m, __builtin_bit_cast(bf16x8, om[m]))                                                          \
        if (m < 6) { P3RD(m + 4) SB(); }
#pragma unroll 1
        for (int it = 0; it < SPIN_MAX; ++it) {
            bf16x8 wf[4][5];
            P3RD(0) P3RD(1) P3RD(2) P3RD(3)
            SB();
            if (it == 0) {
#pragma unroll 1
                for (int k = 0; k < SPIN_MAX * 8; ++k) {
                    u32 v;
                    asm volatile("ds_read_b32 %0, %1\n\ts_waitcnt lgkmcnt(0)" : "=v"(v) : "v"(L_TAG + 4 + ((ts >> 1) + kh * 2) * 4) : "memory");
                    if (uni(v)) break;
                }
            }
            u32x4 om[8]; u32x2 os[4];
#pragma unroll
            for (int m = 0; m < 8; ++m) om[m] = ld4c<0>(vm, ob + m * 2560);
#pragma unroll
            for (int m = 0; m < 4; ++m) os[m] = ld2c<0>(vs, ob + m * 5120);
            u32 mo = 0;
#pragma unroll
            for (int n = 0; n < 5; ++n) acc[n] = f32x4{};
            P3MAIN(0, 11) P3MAIN(1, 10) P3MAIN(2, 9) P3MAIN(3, 8) P3MAIN(4, 7) P3MAIN(5, 6) P3MAIN(6, 5) P3MAIN(7, 4)
            asm volatile("s_waitcnt vmcnt(0)" : "+v"(os[0]), "+v"(os[1]), "+v"(os[2]), "+v"(os[3]) :: "memory");
            mo = mx2(mx2(mx2(mx2(mo, os[0]), os[1]), os[2]), os[3]);
            P3MM(8, cat8(__builtin_bit_cast(s16x4, os[0]), __builtin_bit_cast(s16x4, os[1])))
            P3MM(9, cat8(__builtin_bit_cast(s16x4, os[2]), __builtin_bit_cast(s16x4, os[3])))
            if (__builtin_expect(__builtin_amdgcn_ballot_w64(mo == EMPTY) == 0, 1)) break;
        }
        const u32 ra = L_RED + ts * 5120 + l * 16;
        if (kh) {
            lds_st<f32x4>(ra, acc[0]); lds_st<f32x4>(ra + 1024, acc[1]); lds_st<f32x4>(ra + 2048, acc[4]);
            acc[0] = acc[2]; acc[1] = acc[3];
        } else {
            lds_st<f32x4>(ra + 3072, acc[2]); lds_st<f32x4>(ra + 4096, acc[3]);
        }
        BAR();
        const u32 rb = ra + kh * 3072;
        const float g = bflo(gv), og = rbf(1.0f - g);
        const int slot = lds_ld<int>(L_MAP + tokl * 4);
        u32x2 ov[3];
#pragma unroll
        for (int n = 0; n < 3; ++n) {
            if (n == 2 && kh) break;
            f32x4 a = acc[n == 2 ? 4 : n] + lds_ld<f32x4>(rb + n * 1024);
            u32x2 xv = n == 0 ? xr0 : n == 1 ? xr1 : xr2;
            f32x4 bb = {bflo(xv[0]), bfhi(xv[0]), bflo(xv[1]), bfhi(xv[1])};
            if (slot >= 0) bb += lds_ld<f32x4>(L_YB + slot * 320 + (n == 2 ? 256 : kh * 128 + n * 64) + kg * 16);
            f32x4 rr = g * a + og * bb;
            ov[n][0] = pk2(rr[0], rr[1]); ov[n][1] = pk2(rr[2], rr[3]);
            if (n) {
                const u32x2 &pa = ov[n & 2], &pb = ov[n];
                auto s0 = __builtin_amdgcn_permlane16_swap(pa[0], pb[0], false, false);
                auto s1 = __builtin_amdgcn_permlane16_swap(pa[1], pb[1], false, false);
                u32x4 o4 = {s0[0], s1[0], s0[1], s1[1]};
                if (n == 1) st4p<0>(xo + (kg & 1) * 24, o4, p.out);
                else if (!(kg & 1)) st4p<128>(xo, o4, p.out);
            }
        }
    }
}

extern "C" void kernel_launch(void* const* d_in, const int* in_sizes, int n_in,
                              void* d_out, int out_size, void* d_ws, size_t ws_size,
                              hipStream_t stream) {
    Params p;
    p.x = (const char*)d_in[0]; p.wqkv = (const char*)d_in[1]; p.wo = (const char*)d_in[2];
    p.wr = (const char*)d_in[4]; p.wg = (const char*)d_in[5]; p.wu = (const char*)d_in[6];
    p.wd = (const char*)d_in[7]; p.gw = (const char*)d_in[8];
    p.out = (char*)d_out; p.ws = (char*)d_ws;
    (void)hipFuncSetAttribute((const void*)fused_kernel, hipFuncAttributeMaxDynamicSharedMemorySize, LDS_BYTES);
    fused_kernel<<<256, 768, LDS_BYTES, stream>>>(p);
}
```
